```python
import torch

def _softmax(x, axis=-1):
    return torch.softmax(x, dim=axis)
def _gelu(x):
    return torch.nn.functional.gelu(x, approximate="tanh")
def _silu(x):
    return torch.nn.functional.silu(x)
def _sigmoid(x):
    return torch.sigmoid(x)
def _layernorm(x, axis=-1, eps=1e-5):
    m = x.mean(dim=axis, keepdim=True)
    v = x.var(dim=axis, keepdim=True, unbiased=False)
    return (x - m) / torch.sqrt(v + eps)
def _scatter_add(tgt, idx, src, axis=0):
    return tgt.clone().index_add_(axis, idx, src)
def _segment_sum(src, seg, n):
    out = torch.zeros((n,) + src.shape[1:], dtype=src.dtype)
    return out.index_add_(0, seg, src)
def _pad(x, pw):
    flat = tuple(v for pair in reversed(pw) for v in pair)
    return torch.nn.functional.pad(x, flat)

kernel_name = 'sequential_preLN__ssm_conv_s4d__moe_arch_mod__prompt'

def setup_inputs():

    _w = (lambda a: (lambda t: t.to(torch.bfloat16) if t.is_floating_point() else t)(torch.as_tensor(a)))
    def _A_setup():
        Bsz, L, D, N = 1, 2048, 640, 192
        return {
            "u": _w((lambda *s: torch.randn(*s, dtype=torch.bfloat16) * 0.05)(Bsz, L, D)),
            "log_A_real": _w((lambda *s: torch.randn(*s, dtype=torch.bfloat16) * 6.4)(D, N) * 0.5),
            "A_imag": _w((lambda *s: torch.randn(*s, dtype=torch.bfloat16) * 0.05)(D, N) * 3.14),
            "C_real": _w((lambda *s: torch.randn(*s, dtype=torch.bfloat16) * 0.05)(D, N) / N ** 0.5),
            "C_imag": _w((lambda *s: torch.randn(*s, dtype=torch.bfloat16) * 0.05)(D, N) / N ** 0.5),
            "log_dt": _w((lambda *s: torch.randn(*s, dtype=torch.bfloat16) * 0.05)(D) * 0.5 - 4.0),
            "D_skip": _w((lambda *s: torch.randn(*s, dtype=torch.bfloat16) * 0.05)(D)),
        }
    _A_in = _A_setup()

    def _B_setup():
        B, S, D, F, C = 1, 2048, 640, 1536, 8
        return {
            "x": _w((lambda *s: torch.randn(*s, dtype=torch.bfloat16) * 0.05)(B, S, D)),
            "w_r": _w((lambda *s: torch.randn(*s, dtype=torch.bfloat16) * 0.05)(D)),
            "w_gate": _w((lambda *s: torch.randn(*s, dtype=torch.bfloat16) * 0.05)(D, F)),
            "w_up": _w((lambda *s: torch.randn(*s, dtype=torch.bfloat16) * 0.05)(D, F)),
            "w_down": _w((lambda *s: torch.randn(*s, dtype=torch.bfloat16) * 0.05)(F, D)),
        }
    _B_in = _B_setup()

    return {
        "A_u": _A_in["u"],
        "A_log_A_real": _A_in["log_A_real"],
        "A_A_imag": _A_in["A_imag"],
        "A_C_real": _A_in["C_real"],
        "A_C_imag": _A_in["C_imag"],
        "A_log_dt": _A_in["log_dt"],
        "A_D_skip": _A_in["D_skip"],
        "B_x": _B_in["x"],
        "B_w_r": _B_in["w_r"],
        "B_w_gate": _B_in["w_gate"],
        "B_w_up": _B_in["w_up"],
        "B_w_down": _B_in["w_down"],
    }

def reference(A_u, A_log_A_real, A_A_imag, A_C_real, A_C_imag, A_log_dt, A_D_skip, B_x, B_w_r, B_w_gate, B_w_up, B_w_down):
    def _A_ref(u, log_A_real, A_imag, C_real, C_imag, log_dt, D_skip):
        L = u.shape[1]
        dt = torch.exp(log_dt)
        A = -torch.exp(log_A_real) + 1j * A_imag
        C = (C_real + 1j * C_imag) * dt[:, None]
        pos = (lambda x, dt: x.to(dt))(torch.arange(L), log_dt.dtype)
        P = torch.exp((A * dt[:, None])[:, :, None] * pos[None, None, :])
        K = 2.0 * torch.einsum("dn,dnl->dl", C, P).real
        L2 = 2 * L
        u_t = (lambda x, *d: x.permute(*d))(u, 0, 2, 1)
        Kf = (lambda x, *a, **k: torch.fft.rfft(x.float(), *a, **k))(K, L2)
        Uf = (lambda x, *a, **k: torch.fft.rfft(x.float(), *a, **k))(u_t, L2)
        y = (lambda x, *a, **k: torch.fft.irfft(x, *a, **k).to(torch.bfloat16))(Uf * Kf[None, :, :], L2)[:, :, :L]
        y = (lambda x, *d: x.permute(*d))(y, 0, 2, 1)
        return y + u * D_skip[None, None, :]

    def _B_ref(x, w_r, w_gate, w_up, w_down):
        B, S, D = x.shape
        C = 8
        r = x @ w_r
        idx = torch.argsort(-r, axis=-1)[:, :C]
        idx_e = (lambda x, n, axis: torch.repeat_interleave(x, n, dim=axis))(idx.reshape(B, C, 1), D, axis=2)
        x_sel = (lambda x, i, axis: torch.take_along_dim(x, i, dim=axis))(x, idx_e, axis=1)
        g = _silu(x_sel @ w_gate)
        u = x_sel @ w_up
        y = (g * u) @ w_down
        r_sel = _sigmoid((lambda x, i, axis: torch.take_along_dim(x, i, dim=axis))(r, idx, axis=1))
        y = y * r_sel.reshape(B, C, 1)
        b_off = (torch.arange(B) * S).reshape(B, 1)
        flat_idx = (idx + b_off).reshape(B * C)
        zero = (x * 0.0).reshape(B * S, D)
        upd = _scatter_add(zero, flat_idx, y.reshape(B * C, D), axis=0)
        return x + upd.reshape(B, S, D)

    x = A_u
    h = x + _A_ref(_layernorm(x, axis=-1), A_log_A_real, A_A_imag, A_C_real, A_C_imag, A_log_dt, A_D_skip)
    h2 = h + _B_ref(_layernorm(h, axis=-1), B_w_r, B_w_gate, B_w_up, B_w_down)
    return h2
```

```cpp
#include <hip/hip_runtime.h>
#include <stdint.h>

typedef unsigned int u32;
typedef unsigned long long u64;
typedef unsigned short u16;
typedef u32 v4u __attribute__((ext_vector_type(4)));
typedef u32 v2u __attribute__((ext_vector_type(2)));
typedef float f4 __attribute__((ext_vector_type(4)));
typedef float f2 __attribute__((ext_vector_type(2)));
typedef _Float16 h2 __attribute__((ext_vector_type(2)));
typedef _Float16 h8 __attribute__((ext_vector_type(8)));
typedef __bf16 b2 __attribute__((ext_vector_type(2)));
typedef __bf16 b8 __attribute__((ext_vector_type(8)));
typedef short s4 __attribute__((ext_vector_type(4)));
#define LAS __attribute__((address_space(3)))
#define DEV __device__ __forceinline__

namespace {

constexpr int L = 2048, D = 640, NS = 192, F = 1536;
constexpr int NT = 768;
constexpr u32 SENT = 0xFFFFFFFFu;
constexpr u32 SPIN_CAP = 1u << 16;

constexpr int WS_EPOCH = 0;
constexpr int WS_G1 = 4096;
constexpr int WS_G3 = WS_G1 + 16384;
constexpr int WS_IDX = WS_G3 + 16384;
constexpr int WS_ACT = WS_IDX + 2048;
constexpr int WS_HX = 65536;
constexpr int WS_HROW = WS_HX + L * D * 2;
constexpr int WS_END = WS_HROW + L * D * 2;
constexpr int WS_DBG = 16 << 20;

constexpr int TROW = 800;
constexpr int TCH = 57 * TROW;
constexpr int LD_TBL = 0;
constexpr int KD_LEN = 672;
constexpr int LD_KD = 136800;
constexpr int LD_XS = LD_KD + 3 * KD_LEN * 2;
constexpr int LD_MISC = LD_XS + 3 * L * 2;
constexpr int LD_BYTES = LD_MISC + 1024;
constexpr int KCS = 4416;
constexpr int LD_KC = 0;
constexpr int ULEN = 2320;
constexpr int LD_PART = 53248;
constexpr int LD_HB = 87040;
constexpr int LD_HT = 0;
constexpr int HT_ROW = 648 * 2;
constexpr int M_CLR = LD_MISC;
constexpr int M_SCALE = LD_MISC + 16;
constexpr int M_WMAX = LD_MISC + 64;
constexpr int M_CNT = LD_MISC + 96;
constexpr int M_SEL = LD_MISC + 128;
constexpr int Z0 = 2064;

constexpr int LF_W = 0;
constexpr int LF_CAND = 16384;
constexpr int LF_Z = 32768;
constexpr int LF_H = LF_Z + 8 * HT_ROW;
constexpr int LF_RED = 57344;

constexpr int LD_UU = 99328;

DEV float bf_lo(u32 v) { return __uint_as_float(v << 16); }
DEV float bf_hi(u32 v) { return __uint_as_float(v & 0xffff0000u); }
DEV float bf2f(u16 v) { return __uint_as_float((u32)v << 16); }
DEV u32 pk_bf(float a, float b) {
    f2 v = {a, b};
    return __builtin_bit_cast(u32, __builtin_convertvector(v, b2));
}
DEV h2 as_h2(u32 v) { return __builtin_bit_cast(h2, v); }
DEV float rbf(float a) { return bf_lo(pk_bf(a, a)); }
DEV u32 pk_h(float a, float b) {
    f2 v = {a, b};
    return __builtin_bit_cast(u32, __builtin_convertvector(v, h2));
}
template <int CTRL> DEV float dppf(float v) {
    return __int_as_float(__builtin_amdgcn_update_dpp(0, __float_as_int(v), CTRL, 0xf, 0xf, true));
}
template <int CTRL> DEV u32 dppu(u32 v) { return (u32)__builtin_amdgcn_update_dpp(0, (int)v, CTRL, 0xf, 0xf, true); }
DEV float wave_sum(float v) {
    v += dppf<0xB1>(v);
    v += dppf<0x4E>(v);
    v += dppf<0x141>(v);
    v += dppf<0x140>(v);
    auto r = __builtin_amdgcn_permlane16_swap(__float_as_uint(v), __float_as_uint(v), false, false);
    v = __uint_as_float(r[0]) + __uint_as_float(r[1]);
    auto q = __builtin_amdgcn_permlane32_swap(__float_as_uint(v), __float_as_uint(v), false, false);
    return __uint_as_float(q[0]) + __uint_as_float(q[1]);
}
DEV u32 umax(u32 a, u32 b) { return a > b ? a : b; }
DEV u32 wave_max(u32 v) {
    v = umax(v, dppu<0xB1>(v));
    v = umax(v, dppu<0x4E>(v));
    v = umax(v, dppu<0x141>(v));
    v = umax(v, dppu<0x140>(v));
    auto r = __builtin_amdgcn_permlane16_swap(v, v, false, false);
    v = umax(r[0], r[1]);
    auto q = __builtin_amdgcn_permlane32_swap(v, v, false, false);
    return umax(q[0], q[1]);
}

struct cf { float r, i; };
DEV cf cmul(cf a, cf b) { return {a.r * b.r - a.i * b.i, a.r * b.i + a.i * b.r}; }
DEV cf csq(cf a) { return {a.r * a.r - a.i * a.i, (a.r + a.r) * a.i}; }
DEV cf cexp_pos(float zr, float zi, float pos) {
    const float mag = __builtin_amdgcn_exp2f(zr * pos * 1.4426950408889634f);
    const float rev = __builtin_amdgcn_fractf(zi * pos * 0.15915494309189535f);
    return {mag * __builtin_amdgcn_cosf(rev), mag * __builtin_amdgcn_sinf(rev)};
}
DEV u32 c2h(cf a) { return pk_h(a.r, a.i); }
DEV u32 hcmul(u32 a_, u32 b_) {
    const h2 a = __builtin_bit_cast(h2, a_), b = __builtin_bit_cast(h2, b_);
    const h2 t = h2{a.x, a.x} * b;
    const h2 r = __builtin_elementwise_fma(h2{a.y, a.y}, h2{-b.y, b.x}, t);
    return __builtin_bit_cast(u32, r);
}

DEV void ln_stats(const u32 (&v)[5], float& m, float& s) {
    float sum = 0.f;
#pragma unroll
    for (int i = 0; i < 5; ++i) sum += bf_lo(v[i]) + bf_hi(v[i]);
    const float mean = wave_sum(sum) * (1.0f / D);
    float sq = 0.f;
#pragma unroll
    for (int i = 0; i < 5; ++i) {
        const float a = bf_lo(v[i]) - mean, b = bf_hi(v[i]) - mean;
        sq += a * a + b * b;
    }
    const float var = wave_sum(sq) * (1.0f / D);
    m = rbf(mean);
    s = rbf(__builtin_sqrtf(rbf(rbf(var) + 1e-5f)));
}
DEV float ln_apply(float x, float m, float s, float rs) {
    const float a = rbf(x - m);
    const float q = a * rs;
    const float e = __builtin_fmaf(-q, s, a);
    return rbf(__builtin_fmaf(e, rs, q));
}

DEV v4u ld_sc1(__amdgpu_buffer_rsrc_t rs, int off) { return __builtin_amdgcn_raw_buffer_load_b128(rs, off, 0, 16); }
DEV void st_sc1(__amdgpu_buffer_rsrc_t rs, int off, v4u v) { __builtin_amdgcn_raw_buffer_store_b128(v, rs, off, 0, 16); }
DEV void drain() { asm volatile("s_waitcnt vmcnt(0)" ::: "memory"); }
DEV void cbar() { asm volatile("" ::: "memory"); }

DEV u32 kidx(int lag) {
    if (lag < 0 || lag >= L) return 660;
    const int sh = lag < 256 ? 0 : (24 - __builtin_clz((u32)lag));
    const int k = sh ? ((lag + (1 << (sh - 1)) - 1 + ((lag >> sh) & 1)) >> sh) : lag;
    return (u32)(128 * sh + k);
}

template <int N0, int N1>
DEV void conv_load_frags(LAS const unsigned char* kc, int lane, h8 (&A)[29]) {
    const int q = lane & 15, g = lane >> 4;
    const int s0 = Z0 - q + 8 * g;
    const int sg = s0 & 3;
    LAS const unsigned char* p = kc + sg * KCS + 2 * (s0 - sg);
#pragma unroll
    for (int n = N0; n < N1; ++n) {
        const v2u lo = *(LAS const v2u*)(p - 64 * n);
        const v2u hi = *(LAS const v2u*)(p - 64 * n + 8);
        const v4u t = {lo.x, lo.y, hi.x, hi.y};
        A[n - N0] = __builtin_bit_cast(h8, t);
    }
}
template <int N0, int N1>
DEV void conv_run(const h8 (&A)[29], LAS const unsigned char* ub, f4 (&acc)[8]) {
#pragma unroll
    for (int nu = 8; nu >= -56; --nu) {
        bool any = false;
#pragma unroll
        for (int T = 0; T < 8; ++T) {
            const int n = nu + 8 * T;
            any |= (n >= N0 && n < N1);
        }
        if (!any) continue;
        const h8 B = *(LAS const h8*)(ub - 64 * nu);
#pragma unroll
        for (int T = 0; T < 8; ++T) {
            const int n = nu + 8 * T;
            if (n >= N0 && n < N1) acc[T] = __builtin_amdgcn_mfma_f32_16x16x32_f16(A[n - N0], B, acc[T], 0, 0, 0);
        }
    }
}
DEV constexpr int pslot(int job, int T) {
    return job == 0 ? T - 3 : (job == 1 ? (T <= 2 ? 4 + T : T + 1) : T + 5);
}

}

__global__ void __launch_bounds__(NT, 3)
s4d_moe_fused(const u16* __restrict__ x, const u16* __restrict__ pLogA, const u16* __restrict__ pAim,
              const u16* __restrict__ pCr, const u16* __restrict__ pCi, const u16* __restrict__ pLogDt,
              const u16* __restrict__ pDs, const u16* __restrict__ pWr, const u16* __restrict__ pWg,
              const u16* __restrict__ pWu, const u16* __restrict__ pWd, u16* __restrict__ out,
              unsigned char* __restrict__ ws) {
    __shared__ __attribute__((aligned(16))) unsigned char smem_[160 * 1024];
    LAS unsigned char* sm = (LAS unsigned char*)smem_;
    const int tid = threadIdx.x, lane = tid & 63;
    const int w = __builtin_amdgcn_readfirstlane(tid >> 6);
    const int b = blockIdx.x, grp = b & 7, slot = b >> 3;
    const bool is_conv = slot < 27;
    const int d0 = 80 * grp + 3 * slot;
    const int nch = is_conv ? (slot == 26 ? 2 : 3) : 0;
    const int f = (slot - 27) * 8 + grp;
    const __amdgpu_buffer_rsrc_t rws = __builtin_amdgcn_make_buffer_rsrc((void*)ws, 0, WS_END, 0x00020000);

    if (tid == 0) *(LAS u32*)(sm + M_CLR) = 0;
    __syncthreads();

    u32 xrow[5];
    u32 tag = 0;
    u32 wr[5];
    if (w < 8) {
        const u32* xr = (const u32*)(x + (size_t)(8 * b + w) * D);
#pragma unroll
        for (int i = 0; i < 5; ++i) xrow[i] = xr[lane + 64 * i];
        tag = *(const u32*)(ws + WS_EPOCH) + 1u;
#pragma unroll
        for (int i = 0; i < 5; ++i) wr[i] = ((const u32*)pWr)[lane + 64 * i];
    }

    if (is_conv) {
        const int cs = min(d0 & ~1, D - 4), so = d0 - cs;
        v2u xs[4];
        if (w < 8) {
#pragma unroll
            for (int j = 0; j < 4; ++j) xs[j] = *(const v2u*)(x + (size_t)(4 * tid + j) * D + cs);
        }
        float zr[3], zi[3], cr[3], ci[3];
        {
            const int c = w < 9 ? w / 3 : w - 9;
            const int d = min(d0 + c, D - 1);
            const float dt = rbf(__expf(bf2f(pLogDt[d])));
#pragma unroll
            for (int i = 0; i < 3; ++i) {
                if (w >= 9 || i == 0) {
                    const int n = (w < 9 ? (w % 3) : i) * 64 + lane;
                    const float la = bf2f(pLogA[d * NS + n]), ai = bf2f(pAim[d * NS + n]);
                    zr[i] = -rbf(__expf(la)) * dt;
                    zi[i] = ai * dt;
                    if (w >= 9) {
                        cr[i] = bf2f(pCr[d * NS + n]) * dt;
                        ci[i] = bf2f(pCi[d * NS + n]) * dt;
                    }
                }
            }
        }
        if (w >= 8) {
            const int t = tid - 512;
            const v4u sv = {SENT, SENT, SENT, SENT};
#pragma unroll
            for (int i = 0; i < 3; ++i) {
                const int p = t + 256 * i;
                if (p < D) st_sc1(rws, WS_HX + b * (D * 16) + p * 16, sv);
            }
            if (t < 4) st_sc1(rws, WS_G3 + b * 64 + t * 16, sv);
            drain();
            if (lane == 0) __hip_atomic_fetch_add((LAS u32*)(sm + M_CLR), 1u, __ATOMIC_RELAXED, __HIP_MEMORY_SCOPE_WORKGROUP);
        }
        u32 gi[6], gi2[6];
        {
            const int a1 = 1 + (tid & 255);
            const int e2 = tid & 7;
            const int a2 = e2 == 0 ? 0 : 256 + e2;
#pragma unroll
            for (int k = 0; k < 6; ++k) {
                gi[k] = (kidx(Z0 - 8 * a1 - 2 * k) * 2) | (kidx(Z0 - 8 * a1 - 2 * k - 1) * 2 << 16);
                gi2[k] = (kidx(Z0 - 8 * a2 - 2 * k) * 2) | (kidx(Z0 - 8 * a2 - 2 * k - 1) * 2 << 16);
            }
        }
        if (w < 8) {
            float m, s;
            ln_stats(xrow, m, s);
            if (lane == 0) {
                u32 sp = 0;
                while (__hip_atomic_load((LAS u32*)(sm + M_CLR), __ATOMIC_RELAXED, __HIP_MEMORY_SCOPE_WORKGROUP) < 4u && ++sp < (1u << 20)) {}
                v2u g;
                g.x = (__float_as_uint(m) >> 16) | (__float_as_uint(s) & 0xffff0000u);
                g.y = tag;
                __builtin_amdgcn_raw_buffer_store_b64(g, rws, WS_G1 + (8 * b + w) * 8, 0, 16);
            }
#pragma unroll
            for (int c = 0; c < 3; ++c) {
                const int e = so + c;
                u32 v[4];
#pragma unroll
                for (int j = 0; j < 4; ++j) {
                    const u32 dw = (e & 2) ? xs[j].y : xs[j].x;
                    v[j] = (e & 1) ? (dw >> 16) : (dw & 0xffffu);
                }
                const v2u pkd = {v[0] | (v[1] << 16), v[2] | (v[3] << 16)};
                *(LAS v2u*)(sm + LD_XS + c * (L * 2) + tid * 8) = pkd;
            }
        }
        if (w < 9) {
            const int c = w / 3, n = (w % 3) * 64 + lane;
            LAS u32* T = (LAS u32*)(sm + LD_TBL + c * TCH + n * 4);
            constexpr int RS = TROW / 4;
            const cf B1 = cexp_pos(zr[0], zi[0], 1.f), H1 = cexp_pos(zr[0], zi[0], 128.f);
            const cf B2 = csq(B1), B3 = cmul(B2, B1), B4 = csq(B2), B5 = cmul(B4, B1), B6 = csq(B3), B7 = cmul(B4, B3);
            u32 hb[8];
            hb[0] = 0x00003C00u;
            hb[1] = c2h(B1); hb[2] = c2h(B2); hb[3] = c2h(B3); hb[4] = c2h(B4); hb[5] = c2h(B5); hb[6] = c2h(B6); hb[7] = c2h(B7);
            const cf H2 = csq(H1), H3 = cmul(H2, H1), H4 = csq(H2), H5 = cmul(H4, H1), H6 = csq(H3), H7 = cmul(H4, H3), H8 = csq(H4);
            const u32 h1 = c2h(H1), h2_ = c2h(H2), h3 = c2h(H3), h4 = c2h(H4), h5 = c2h(H5), h6 = c2h(H6), h7 = c2h(H7);
#pragma unroll
            for (int k = 0; k < 8; ++k) T[(16 + k) * RS] = hb[k];
            T[24 * RS] = h1;
#pragma unroll
            for (int k = 1; k < 8; ++k) T[(24 + k) * RS] = hcmul(h1, hb[k]);
            T[32 * RS] = h2_; T[33 * RS] = hcmul(h2_, hb[2]); T[34 * RS] = hcmul(h2_, hb[4]); T[35 * RS] = hcmul(h2_, hb[6]);
            T[36 * RS] = h3;  T[37 * RS] = hcmul(h3, hb[2]);  T[38 * RS] = hcmul(h3, hb[4]);  T[39 * RS] = hcmul(h3, hb[6]);
            T[40 * RS] = h4;  T[41 * RS] = hcmul(h4, hb[4]);
            T[42 * RS] = h5;  T[43 * RS] = hcmul(h5, hb[4]);
            T[44 * RS] = h6;  T[45 * RS] = hcmul(h6, hb[4]);
            T[46 * RS] = h7;  T[47 * RS] = hcmul(h7, hb[4]);
            T[48 * RS] = c2h(H8);
            T[49 * RS] = c2h(cmul(H8, H1));
            T[50 * RS] = c2h(csq(H5));
            T[51 * RS] = c2h(cmul(H8, H3));
            T[52 * RS] = c2h(csq(H6));
            T[53 * RS] = c2h(cmul(H8, H5));
            T[54 * RS] = c2h(csq(H7));
            T[55 * RS] = c2h(cmul(H8, H7));
            T[56 * RS] = c2h(csq(H8));
        } else {
            const int c = w - 9;
            float sabs = 0.f;
#pragma unroll
            for (int i = 0; i < 3; ++i) sabs += __builtin_fabsf(cr[i]) + __builtin_fabsf(ci[i]);
            sabs = wave_sum(sabs);
            int ex = __builtin_amdgcn_frexp_expf(sabs);
            int e = 14 - ex;
            e = e > 100 ? 100 : (e < -100 ? -100 : e);
            const float sc = __builtin_ldexpf(1.f, e + 1);
            if (lane == 0) *(LAS float*)(sm + M_SCALE + 4 * c) = __builtin_ldexpf(1.f, -e);
            constexpr int RS = TROW / 4;
#pragma unroll
            for (int i = 0; i < 3; ++i) {
                LAS u32* T = (LAS u32*)(sm + LD_TBL + c * TCH + (i * 64 + lane) * 4);
                const cf w8 = cexp_pos(zr[i], zi[i], 8.f);
                cf be = {cr[i] * sc, ci[i] * sc};
#pragma unroll
                for (int j = 0; j < 16; ++j) {
                    T[j * RS] = pk_h(be.r, -be.i);
                    if (j < 15) be = cmul(be, w8);
                }
            }
            if (lane < 16) *(LAS u16*)(sm + LD_KD + c * (KD_LEN * 2) + (656 + lane) * 2) = 0;
        }
        __syncthreads();
        if (w < 9) {
            const int c = w / 3, tile = w % 3;
            const int j = lane & 15, g = lane >> 4;
            int row = 16 + 16 * tile + j;
            if (row > 56) row = 56;
            LAS const unsigned char* pa = sm + LD_TBL + c * TCH + row * TROW + g * 16;
            LAS const unsigned char* pb = sm + LD_TBL + c * TCH + j * TROW + g * 16;
            f4 acc = {0.f, 0.f, 0.f, 0.f};
#pragma unroll
            for (int t = 0; t < 12; ++t)
                acc = __builtin_amdgcn_mfma_f32_16x16x32_f16(*(LAS const h8*)(pa + 64 * t), *(LAS const h8*)(pb + 64 * t), acc, 0, 0, 0);
            const u32 p01 = pk_h(acc[0], acc[1]), p23 = pk_h(acc[2], acc[3]);
            LAS unsigned char* kd = sm + LD_KD + c * (KD_LEN * 2);
            if (tile == 0) {
                *(LAS v2u*)(kd + 2 * (128 * (g >> 1) + 4 * (g & 1) + 8 * j)) = v2u{p01, p23};
            } else if (tile == 1) {
                if (g < 2) {
                    *(LAS v2u*)(kd + 2 * (256 + 64 * g + 4 * j)) = v2u{p01, p23};
                } else {
                    *(LAS u32*)(kd + 2 * (384 + 64 * (g - 2) + 2 * j)) = p01;
                    *(LAS u32*)(kd + 2 * (384 + 64 * (g - 2) + 32 + 2 * j)) = p23;
                }
            } else {
                const int r0 = 4 * g;
                if (r0 <= 8) *(LAS u16*)(kd + 2 * (512 + 16 * r0 + j)) = (u16)p01;
                if (r0 + 3 <= 8) {
                    *(LAS u16*)(kd + 2 * (512 + 16 * (r0 + 1) + j)) = (u16)(p01 >> 16);
                    *(LAS u16*)(kd + 2 * (512 + 16 * (r0 + 2) + j)) = (u16)p23;
                    *(LAS u16*)(kd + 2 * (512 + 16 * (r0 + 3) + j)) = (u16)(p23 >> 16);
                }
            }
        }
        __syncthreads();
        {
            auto expand = [&](int c, int a, const u32 (&ix)[6]) {
                LAS const unsigned char* kd = sm + LD_KD + c * (KD_LEN * 2);
                u32 P[6];
#pragma unroll
                for (int k = 0; k < 6; ++k)
                    P[k] = (u32) * (LAS const u16*)(kd + (ix[k] & 0xffffu)) | ((u32) * (LAS const u16*)(kd + (ix[k] >> 16)) << 16);
                LAS unsigned char* dst = sm + LD_KC + c * (4 * KCS) + a * 16;
                *(LAS v4u*)(dst) = v4u{P[0], P[1], P[2], P[3]};
                *(LAS v4u*)(dst + KCS) = v4u{__builtin_amdgcn_alignbit(P[1], P[0], 16), __builtin_amdgcn_alignbit(P[2], P[1], 16),
                                             __builtin_amdgcn_alignbit(P[3], P[2], 16), __builtin_amdgcn_alignbit(P[4], P[3], 16)};
                *(LAS v4u*)(dst + 2 * KCS) = v4u{P[1], P[2], P[3], P[4]};
                *(LAS v4u*)(dst + 3 * KCS) = v4u{__builtin_amdgcn_alignbit(P[2], P[1], 16), __builtin_amdgcn_alignbit(P[3], P[2], 16),
                                                 __builtin_amdgcn_alignbit(P[4], P[3], 16), __builtin_amdgcn_alignbit(P[5], P[4], 16)};
            };
            expand(tid >> 8, 1 + (tid & 255), gi);
            if (tid < 24) expand(tid >> 3, (tid & 7) == 0 ? 0 : 256 + (tid & 7), gi2);
            if (tid < 102) {
                const int c = tid / 34, k = tid % 34;
                const int el = k < 32 ? 8 * k : 2304 + 8 * (k - 32);
                *(LAS v4u*)(sm + LD_UU + c * (ULEN * 2) + el * 2) = v4u{0, 0, 0, 0};
            }
        }
        __syncthreads();
        int job = -1, jc = 0;
        if ((w & 3) == 0) { job = 2; jc = w >> 2; }
        else if (w < 8) { job = (w >> 2); jc = (w & 3) - 1; }
        h8 A[29];
        LAS const unsigned char* kc = sm + LD_KC + jc * (4 * KCS);
        if (job == 0) conv_load_frags<0, 16>(kc, lane, A);
        else if (job == 1) conv_load_frags<16, 36>(kc, lane, A);
        else if (job == 2) conv_load_frags<36, 65>(kc, lane, A);

        if (w < 8) {
            v4u g0, g1;
            u32 sp = 0;
            while (true) {
                cbar();
                g0 = ld_sc1(rws, WS_G1 + tid * 32);
                g1 = ld_sc1(rws, WS_G1 + tid * 32 + 16);
                const bool ok = (g0.y == tag) & (g0.w == tag) & (g1.y == tag) & (g1.w == tag);
                if (__all(ok) || ++sp > SPIN_CAP) break;
            }
            const u32 ms[4] = {g0.x, g0.z, g1.x, g1.z};
            float mm[4], ss[4], rs[4];
#pragma unroll
            for (int j = 0; j < 4; ++j) {
                mm[j] = bf_lo(ms[j]); ss[j] = bf_hi(ms[j]); rs[j] = __builtin_amdgcn_rcpf(ss[j]);
            }
#pragma unroll
            for (int c = 0; c < 3; ++c) {
                const int e = so + c;
                float u[4];
#pragma unroll
                for (int j = 0; j < 4; ++j) {
                    const u32 dw = (e & 2) ? xs[j].y : xs[j].x;
                    const float xv = (e & 1) ? bf_hi(dw) : bf_lo(dw);
                    u[j] = ln_apply(xv, mm[j], ss[j], rs[j]);
                }
                *(LAS v2u*)(sm + LD_UU + c * (ULEN * 2) + (256 + 4 * tid) * 2) = v2u{pk_h(u[0], u[1]), pk_h(u[2], u[3])};
            }
        }
        __syncthreads();
        f4 acc[8];
#pragma unroll
        for (int T = 0; T < 8; ++T) acc[T] = f4{0.f, 0.f, 0.f, 0.f};
        {
            const int p = lane & 15, g = lane >> 4;
            LAS const unsigned char* ub = sm + LD_UU + jc * (ULEN * 2) + (256 + 16 * p + 8 * g) * 2;
            LAS unsigned char* part = sm + LD_PART + jc * (11 * 1024) + lane * 16;
            if (job == 0) {
                conv_run<0, 16>(A, ub, acc);
#pragma unroll
                for (int T = 3; T < 8; ++T) *(LAS f4*)(part + pslot(0, T) * 1024) = acc[T];
            } else if (job == 1) {
                conv_run<16, 36>(A, ub, acc);
                *(LAS f4*)(part + pslot(1, 1) * 1024) = acc[1];
                *(LAS f4*)(part + pslot(1, 2) * 1024) = acc[2];
                *(LAS f4*)(part + pslot(1, 6) * 1024) = acc[6];
                *(LAS f4*)(part + pslot(1, 7) * 1024) = acc[7];
            } else if (job == 2) {
                conv_run<36, 65>(A, ub, acc);
                *(LAS f4*)(part + pslot(2, 4) * 1024) = acc[4];
                *(LAS f4*)(part + pslot(2, 5) * 1024) = acc[5];
            }
        }
        __syncthreads();
        if (job >= 0 && jc < nch) {
            const int p = lane & 15, g = lane >> 4;
            const float isc = *(LAS const float*)(sm + M_SCALE + 4 * jc);
            const float dsk = bf2f(pDs[d0 + jc]);
            LAS const unsigned char* part = sm + LD_PART + jc * (11 * 1024) + lane * 16;
            auto fin = [&](int T, f4 y) {
                const int tok = 256 * T + 16 * p + 4 * g;
                const v2u uu = *(LAS const v2u*)(sm + LD_UU + jc * (ULEN * 2) + (256 + tok) * 2);
                const v2u xx = *(LAS const v2u*)(sm + LD_XS + jc * (L * 2) + tok * 2);
                const h2 u01 = as_h2(uu.x), u23 = as_h2(uu.y);
                const float uf[4] = {(float)u01.x, (float)u01.y, (float)u23.x, (float)u23.y};
                const float xf[4] = {bf_lo(xx.x), bf_hi(xx.x), bf_lo(xx.y), bf_hi(xx.y)};
                float h[4];
#pragma unroll
                for (int r = 0; r < 4; ++r) h[r] = xf[r] + rbf(rbf(y[r] * isc) + rbf(uf[r] * dsk));
                *(LAS v2u*)(sm + LD_HB + jc * (L * 2) + tok * 2) = v2u{pk_bf(h[0], h[1]), pk_bf(h[2], h[3])};
            };
            auto P = [&](int j, int T) { return *(LAS const f4*)(part + pslot(j, T) * 1024); };
            if (job == 0) {
                fin(0, acc[0]);
                fin(1, acc[1] + P(1, 1));
                fin(2, acc[2] + P(1, 2));
            } else if (job == 1) {
                fin(3, acc[3] + P(0, 3));
                fin(4, acc[4] + P(0, 4) + P(2, 4));
                fin(5, acc[5] + P(0, 5) + P(2, 5));
            } else {
                fin(6, acc[6] + P(0, 6) + P(1, 6));
                fin(7, acc[7] + P(0, 7) + P(1, 7));
            }
        }
        __syncthreads();
        {
            const int c = tid >> 8, o = tid & 255;
            if (c < nch) {
                const v4u v = *(LAS const v4u*)(sm + LD_HB + c * (L * 2) + o * 16);
                st_sc1(rws, WS_HX + o * (D * 16) + (d0 + c) * 16, v);
            }
        }
    } else {
        if (w >= 8) {
            const int t = tid - 512;
            const v4u sv = {SENT, SENT, SENT, SENT};
#pragma unroll
            for (int i = 0; i < 3; ++i) {
                const int p = t + 256 * i;
                if (p < D) st_sc1(rws, WS_HX + b * (D * 16) + p * 16, sv);
            }
            if (t < 4) st_sc1(rws, WS_G3 + b * 64 + t * 16, sv);
            if (f < 32 && t >= 64 && t < 112) {
                const int k = t - 64;
                st_sc1(rws, WS_ACT + (k / 6) * (F * 2) + f * 96 + (k % 6) * 16, sv);
            }
            if (f < 8 && t >= 128 && t < 130) st_sc1(rws, WS_IDX + f * 256 + (t - 128) * 16, sv);
            drain();
            if (lane == 0) __hip_atomic_fetch_add((LAS u32*)(sm + M_CLR), 1u, __ATOMIC_RELAXED, __HIP_MEMORY_SCOPE_WORKGROUP);
        }
        if (w < 8) {
            float m, s;
            ln_stats(xrow, m, s);
            if (lane == 0) {
                u32 sp = 0;
                while (__hip_atomic_load((LAS u32*)(sm + M_CLR), __ATOMIC_RELAXED, __HIP_MEMORY_SCOPE_WORKGROUP) < 4u && ++sp < (1u << 20)) {}
                v2u g;
                g.x = (__float_as_uint(m) >> 16) | (__float_as_uint(s) & 0xffff0000u);
                g.y = tag;
                __builtin_amdgcn_raw_buffer_store_b64(g, rws, WS_G1 + (8 * b + w) * 8, 0, 16);
            }
        }
    }

    v4u wgu[10], wdn[4];
    if (!is_conv) {
        v4u rg[5], ru[5], rd[4];
        if (f < 32) {
#pragma unroll
            for (int i = 0; i < 5; ++i) {
                const int p = tid + NT * i, k = p / 6, q = p % 6;
                rg[i] = *(const v4u*)(pWg + (size_t)k * F + 48 * f + 8 * q);
                ru[i] = *(const v4u*)(pWu + (size_t)k * F + 48 * f + 8 * q);
            }
        }
#pragma unroll
        for (int i = 0; i < 4; ++i) {
            const int p = tid + NT * i, k = p >> 1, q = p & 1;
            rd[i] = *(const v4u*)(pWd + (size_t)k * D + 16 * f + 8 * q);
        }
        if (f < 32) {
#pragma unroll
            for (int i = 0; i < 5; ++i) {
                const int p = tid + NT * i;
                *(LAS v4u*)(sm + LF_W + p * 16) = rg[i];
                *(LAS v4u*)(sm + LF_W + 61440 + p * 16) = ru[i];
            }
        }
        __syncthreads();
        const int g = lane >> 4, q = (lane & 15) >> 2, pp = lane & 3;
        if (f < 32) {
            const int i6 = w % 6, kh = w / 6;
            LAS const unsigned char* base = sm + LF_W + (i6 / 3) * 61440 + (i6 % 3) * 32 + pp * 8;
#pragma unroll
            for (int s = 0; s < 10; ++s) {
                const int k0 = 32 * (10 * kh + s) + 8 * g + q;
                const s4 a = __builtin_amdgcn_ds_read_tr16_b64_v4i16((LAS s4*)(base + k0 * 96));
                const s4 c = __builtin_amdgcn_ds_read_tr16_b64_v4i16((LAS s4*)(base + (k0 + 4) * 96));
                const v2u a2 = __builtin_bit_cast(v2u, a), c2 = __builtin_bit_cast(v2u, c);
                wgu[s] = v4u{a2.x, a2.y, c2.x, c2.y};
            }
        }
        __syncthreads();
#pragma unroll
        for (int i = 0; i < 4; ++i) *(LAS v4u*)(sm + LF_W + (tid + NT * i) * 16) = rd[i];
        __syncthreads();
        {
            LAS const unsigned char* base = sm + LF_W + pp * 8;
#pragma unroll
            for (int s = 0; s < 4; ++s) {
                const int k0 = 32 * (4 * w + s) + 8 * g + q;
                const s4 a = __builtin_amdgcn_ds_read_tr16_b64_v4i16((LAS s4*)(base + k0 * 32));
                const s4 c = __builtin_amdgcn_ds_read_tr16_b64_v4i16((LAS s4*)(base + (k0 + 4) * 32));
                const v2u a2 = __builtin_bit_cast(v2u, a), c2 = __builtin_bit_cast(v2u, c);
                wdn[s] = v4u{a2.x, a2.y, c2.x, c2.y};
            }
        }
    }
    __syncthreads();

    if (tid < D) {
        v4u v;
        u32 sp = 0;
        while (true) {
            cbar();
            v = ld_sc1(rws, WS_HX + b * (D * 16) + tid * 16);
            const bool ok = (v.x != SENT) & (v.y != SENT) & (v.z != SENT) & (v.w != SENT);
            if (__all(ok) || ++sp > SPIN_CAP) break;
        }
        const bool odd = tid & 1;
        const u32 s0 = odd ? v.x : v.z, s1 = odd ? v.y : v.w;
        const u32 r0 = dppu<0xB1>(s0), r1 = dppu<0xB1>(s1);
        const u32 k0 = odd ? v.z : v.x, k1 = odd ? v.w : v.y;
        const u32 lo0 = odd ? r0 : k0, hi0 = odd ? k0 : r0, lo1 = odd ? r1 : k1, hi1 = odd ? k1 : r1;
        LAS u32* dst = (LAS u32*)(sm + LD_HT + (odd ? 4 * HT_ROW : 0) + (tid >> 1) * 4);
        dst[0 * (HT_ROW / 4)] = __builtin_amdgcn_perm(hi0, lo0, 0x05040100u);
        dst[1 * (HT_ROW / 4)] = __builtin_amdgcn_perm(hi0, lo0, 0x07060302u);
        dst[2 * (HT_ROW / 4)] = __builtin_amdgcn_perm(hi1, lo1, 0x05040100u);
        dst[3 * (HT_ROW / 4)] = __builtin_amdgcn_perm(hi1, lo1, 0x07060302u);
    }
    __syncthreads();
    u32 orow[5];
    const int mytok = 8 * b + w;
    if (w < 8) {
        {
            LAS const unsigned char* hr = sm + LD_HT + w * HT_ROW;
            const v4u a = *(LAS const v4u*)(hr + lane * 16);
            st_sc1(rws, WS_HROW + mytok * (D * 2) + lane * 16, a);
            if (lane < 16) {
                const v4u c = *(LAS const v4u*)(hr + 1024 + lane * 16);
                st_sc1(rws, WS_HROW + mytok * (D * 2) + 1024 + lane * 16, c);
            }
        }
        u32 hv[5];
#pragma unroll
        for (int i = 0; i < 5; ++i) hv[i] = *(LAS const u32*)(sm + LD_HT + w * HT_ROW + (lane + 64 * i) * 4);
        float m, s;
        ln_stats(hv, m, s);
        const float rs = __builtin_amdgcn_rcpf(s);
        float racc = 0.f;
#pragma unroll
        for (int i = 0; i < 5; ++i) {
            const float h0 = bf_lo(hv[i]), h1 = bf_hi(hv[i]);
            const float z0 = ln_apply(h0, m, s, rs), z1 = ln_apply(h1, m, s, rs);
            racc += z0 * bf_lo(wr[i]) + z1 * bf_hi(wr[i]);
            orow[i] = pk_bf(h0 + z0, h1 + z1);
        }
        const float r = wave_sum(racc);
        drain();
        if (lane == 0) {
            v2u g;
            g.x = (__float_as_uint(m) >> 16) | (__float_as_uint(s) & 0xffff0000u);
            g.y = pk_bf(r, 0.f) & 0xffffu;
            __builtin_amdgcn_raw_buffer_store_b64(g, rws, WS_G3 + mytok * 8, 0, 16);
        }
    }

    if (!is_conv) {
        u32 key[4], msv[4];
        if (w < 8) {
            v4u g0, g1;
            u32 sp = 0;
            while (true) {
                cbar();
                g0 = ld_sc1(rws, WS_G3 + tid * 32);
                g1 = ld_sc1(rws, WS_G3 + tid * 32 + 16);
                const bool ok = (g0.y != SENT) & (g0.w != SENT) & (g1.y != SENT) & (g1.w != SENT);
                if (__all(ok) || ++sp > SPIN_CAP) break;
            }
            const u32 rb[4] = {g0.y, g0.w, g1.y, g1.w};
            msv[0] = g0.x; msv[1] = g0.z; msv[2] = g1.x; msv[3] = g1.z;
#pragma unroll
            for (int j = 0; j < 4; ++j) {
                const u32 o = (rb[j] & 0x8000u) ? (~rb[j] & 0xffffu) : (rb[j] | 0x8000u);
                key[j] = (o << 11) | (u32)(2047 - (4 * tid + j));
            }
            const u32 mx = wave_max(umax(umax(key[0], key[1]), umax(key[2], key[3])));
            if (lane == 0) *(LAS u32*)(sm + M_WMAX + 4 * w) = mx;
        }
        __syncthreads();
        if (w < 8) {
            const v4u a = *(LAS const v4u*)(sm + M_WMAX), c = *(LAS const v4u*)(sm + M_WMAX + 16);
            const u32 thr = min(min(min(a.x, a.y), min(a.z, a.w)), min(min(c.x, c.y), min(c.z, c.w)));
            int base = 0;
#pragma unroll
            for (int j = 0; j < 4; ++j) {
                const bool p = key[j] >= thr;
                const u64 bal = __ballot(p);
                const int pos = base + __builtin_amdgcn_mbcnt_hi((u32)(bal >> 32), __builtin_amdgcn_mbcnt_lo((u32)bal, 0));
                if (p) *(LAS u32*)(sm + LF_CAND + w * 1024 + pos * 4) = key[j];
                base += __builtin_popcountll(bal);
            }
            if (lane == 0) *(LAS u32*)(sm + M_CNT + 4 * w) = base;
        }
        __syncthreads();
        if (w < 8) {
            const v4u ca = *(LAS const v4u*)(sm + M_CNT), cb = *(LAS const v4u*)(sm + M_CNT + 16);
            const u32 cnt[8] = {ca.x, ca.y, ca.z, ca.w, cb.x, cb.y, cb.z, cb.w};
            const v4u a = *(LAS const v4u*)(sm + M_WMAX), c = *(LAS const v4u*)(sm + M_WMAX + 16);
            const u32 thr = min(min(min(a.x, a.y), min(a.z, a.w)), min(min(c.x, c.y), min(c.z, c.w)));
            const u32 mymax = umax(umax(key[0], key[1]), umax(key[2], key[3]));
            if (__any(mymax >= thr)) {
                u32 rank[4] = {0, 0, 0, 0};
#pragma unroll
                for (int ww = 0; ww < 8; ++ww)
                    for (u32 i = 0; i < cnt[ww]; ++i) {
                        const u32 o = *(LAS const u32*)(sm + LF_CAND + ww * 1024 + i * 4);
#pragma unroll
                        for (int j = 0; j < 4; ++j) rank[j] += o > key[j];
                    }
#pragma unroll
                for (int j = 0; j < 4; ++j)
                    if (key[j] >= thr && rank[j] < 8) {
                        const u32 rbits = key[j] >> 11;
                        const u32 rb = (rbits & 0x8000u) ? (rbits & 0x7fffu) : (~rbits & 0xffffu);
                        *(LAS v4u*)(sm + M_SEL + rank[j] * 16) = v4u{(u32)(4 * tid + j), msv[j], rb, 0u};
                    }
            }
        }
        __syncthreads();
        if (f < 8 && tid < 2) {
            const v4u s0 = *(LAS const v4u*)(sm + M_SEL + tid * 64), s1 = *(LAS const v4u*)(sm + M_SEL + tid * 64 + 16),
                      s2 = *(LAS const v4u*)(sm + M_SEL + tid * 64 + 32), s3 = *(LAS const v4u*)(sm + M_SEL + tid * 64 + 48);
            st_sc1(rws, WS_IDX + f * 256 + tid * 16, v4u{s0.x, s1.x, s2.x, s3.x});
        }
        if (tid < D) {
            const int k = tid / 80, ch = tid % 80;
            const v4u sel = *(LAS const v4u*)(sm + M_SEL + k * 16);
            const v4u hv = ld_sc1(rws, WS_HROW + (int)(sel.x & (L - 1)) * (D * 2) + ch * 16);
            const float m = bf_lo(sel.y), s = bf_hi(sel.y), rs = __builtin_amdgcn_rcpf(s);
            const u32 hh[4] = {hv.x, hv.y, hv.z, hv.w};
            u32 zz[4];
#pragma unroll
            for (int i = 0; i < 4; ++i) zz[i] = pk_bf(ln_apply(bf_lo(hh[i]), m, s, rs), ln_apply(bf_hi(hh[i]), m, s, rs));
            *(LAS v4u*)(sm + LF_Z + k * HT_ROW + ch * 16) = v4u{zz[0], zz[1], zz[2], zz[3]};
            *(LAS v4u*)(sm + LF_H + k * HT_ROW + ch * 16) = hv;
        }
        __syncthreads();
        const int row = lane & 15, g = lane >> 4;
        if (f < 32) {
            const int kh = w / 6;
            f4 acc = {0.f, 0.f, 0.f, 0.f};
#pragma unroll
            for (int s = 0; s < 10; ++s) {
                v4u a = {0, 0, 0, 0};
                if (row < 8) a = *(LAS const v4u*)(sm + LF_Z + row * HT_ROW + (32 * (10 * kh + s) + 8 * g) * 2);
                acc = __builtin_amdgcn_mfma_f32_16x16x32_bf16(__builtin_bit_cast(b8, a), __builtin_bit_cast(b8, wgu[s]), acc, 0, 0, 0);
            }
            *(LAS f4*)(sm + LF_RED + w * 1024 + lane * 16) = acc;
            __syncthreads();
            if (tid < 192) {
                const int tok = tid / 24, cp = tid % 24, tile = cp >> 3, col = (cp & 7) * 2;
                float a[2];
#pragma unroll
                for (int e = 0; e < 2; ++e) {
                    const int off = ((tok >> 2) * 16 + col + e) * 16 + (tok & 3) * 4;
                    const float gs = *(LAS const float*)(sm + LF_RED + tile * 1024 + off) + *(LAS const float*)(sm + LF_RED + (6 + tile) * 1024 + off);
                    const float us = *(LAS const float*)(sm + LF_RED + (3 + tile) * 1024 + off) + *(LAS const float*)(sm + LF_RED + (9 + tile) * 1024 + off);
                    const float gb = rbf(gs);
                    const float sg = rbf(gb / (1.f + __expf(-gb)));
                    a[e] = sg * rbf(us);
                }
                __builtin_amdgcn_raw_buffer_store_b32(pk_bf(a[0], a[1]), rws, WS_ACT + tok * (F * 2) + (48 * f + 16 * tile + col) * 2, 0, 16);
            }
        }
        {
            v4u a[4];
            u32 sp = 0;
            while (true) {
                cbar();
                bool ok = true;
#pragma unroll
                for (int s = 0; s < 4; ++s) {
                    a[s] = v4u{0, 0, 0, 0};
                    if (row < 8) a[s] = ld_sc1(rws, WS_ACT + row * (F * 2) + (32 * (4 * w + s) + 8 * g) * 2);
                    ok &= (a[s].x != SENT) & (a[s].y != SENT) & (a[s].z != SENT) & (a[s].w != SENT);
                }
                if (__all(ok) || ++sp > SPIN_CAP) break;
            }
            f4 acc = {0.f, 0.f, 0.f, 0.f};
#pragma unroll
            for (int s = 0; s < 4; ++s)
                acc = __builtin_amdgcn_mfma_f32_16x16x32_bf16(__builtin_bit_cast(b8, a[s]), __builtin_bit_cast(b8, wdn[s]), acc, 0, 0, 0);
            __syncthreads();
            *(LAS f4*)(sm + LF_RED + w * 1024 + lane * 16) = acc;
            __syncthreads();
            if (tid < 128) {
                const int tok = tid >> 4, col = tid & 15;
                const int off = ((tok >> 2) * 16 + col) * 16 + (tok & 3) * 4;
                float y = 0.f;
#pragma unroll
                for (int ww = 0; ww < 12; ++ww) y += *(LAS const float*)(sm + LF_RED + ww * 1024 + off);
                const v4u sel = *(LAS const v4u*)(sm + M_SEL + tok * 16);
                const float r = bf2f((u16)sel.z);
                const float sg = rbf(1.f / (1.f + __expf(-r)));
                const float upd = rbf(rbf(y) * sg);
                const int d = 16 * f + col;
                const float z = bf2f(*(LAS const u16*)(sm + LF_Z + tok * HT_ROW + d * 2));
                const float h = bf2f(*(LAS const u16*)(sm + LF_H + tok * HT_ROW + d * 2));
                out[(size_t)(sel.x & (L - 1)) * D + d] = (u16)pk_bf(h + rbf(z + upd), 0.f);
            }
        }
    }

    if (w < 8) {
        v4u i0, i1;
        u32 sp = 0;
        while (true) {
            cbar();
            i0 = ld_sc1(rws, WS_IDX + (b & 7) * 256);
            i1 = ld_sc1(rws, WS_IDX + (b & 7) * 256 + 16);
            const bool ok = (i0.x != SENT) & (i0.y != SENT) & (i0.z != SENT) & (i0.w != SENT) & (i1.x != SENT) & (i1.y != SENT) &
                            (i1.z != SENT) & (i1.w != SENT);
            if (__all(ok) || ++sp > SPIN_CAP) break;
            __builtin_amdgcn_s_sleep(8);
        }
        const u32 t = (u32)mytok;
        const bool routed = (i0.x == t) | (i0.y == t) | (i0.z == t) | (i0.w == t) | (i1.x == t) | (i1.y == t) | (i1.z == t) | (i1.w == t);
        if (!routed) {
            u32* o = (u32*)(out + (size_t)mytok * D);
#pragma unroll
            for (int i = 0; i < 5; ++i) o[lane + 64 * i] = orow[i];
        }
        if (b == 0 && tid == 0) *(u32*)(ws + WS_EPOCH) = tag;
    }
}

extern "C" void kernel_launch(void* const* d_in, const int* in_sizes, int n_in, void* d_out, int out_size, void* d_ws,
                              size_t ws_size, hipStream_t stream) {
    (void)in_sizes; (void)n_in; (void)out_size; (void)ws_size;
    s4d_moe_fused<<<256, NT, 0, stream>>>((const u16*)d_in[0], (const u16*)d_in[1], (const u16*)d_in[2], (const u16*)d_in[3],
                                          (const u16*)d_in[4], (const u16*)d_in[5], (const u16*)d_in[6],
                                          (const u16*)d_in[8], (const u16*)d_in[9], (const u16*)d_in[10],
                                          (const u16*)d_in[11], (u16*)d_out, (unsigned char*)d_ws);
}
```

```cpp
#include <hip/hip_runtime.h>
#include <stdint.h>

typedef unsigned int u32;
typedef unsigned long long u64;
typedef unsigned short u16;
typedef u32 v4u __attribute__((ext_vector_type(4)));
typedef u32 v2u __attribute__((ext_vector_type(2)));
typedef float f4 __attribute__((ext_vector_type(4)));
typedef float f2 __attribute__((ext_vector_type(2)));
typedef _Float16 h2 __attribute__((ext_vector_type(2)));
typedef _Float16 h8 __attribute__((ext_vector_type(8)));
typedef __bf16 b2 __attribute__((ext_vector_type(2)));
typedef __bf16 b8 __attribute__((ext_vector_type(8)));
typedef short s4 __attribute__((ext_vector_type(4)));
#define LAS __attribute__((address_space(3)))
#define DEV __device__ __forceinline__

namespace {

constexpr int L = 2048, D = 640, NS = 192, F = 1536;
constexpr int NT = 768;
constexpr u32 SENT = 0xFFFFFFFFu;
constexpr u32 SPIN_CAP = 1u << 16;

constexpr int WS_EPOCH = 0;
constexpr int WS_G1 = 4096;
constexpr int WS_G3 = WS_G1 + 8192;
constexpr int WS_IDX = WS_G3 + 16384;
constexpr int WS_ACT = WS_IDX + 2048;
constexpr int WS_UX = 65536;
constexpr int WS_HX = WS_UX + L * D * 2;
constexpr int WS_HROW = WS_HX + L * D * 2;
constexpr int WS_END = WS_HROW + L * D * 2;
constexpr int WS_DBG = 16 << 20;

constexpr int TROW = 800;
constexpr int TCH = 57 * TROW;
constexpr int LD_TBL = 0;
constexpr int KD_LEN = 672;
constexpr int LD_KD = 136800;
constexpr int LD_UT = LD_KD + 3 * KD_LEN * 2;
constexpr int LD_MISC = 153600;
constexpr int KCS = 4416;
constexpr int LD_KC = 0;
constexpr int ULEN = 2320;
constexpr int LD_PART = 53248;
constexpr int LD_HB = 87040;
constexpr int LD_UU = 99328;
constexpr int LD_HT = 0;
constexpr int HT_ROW = 648 * 2;
constexpr int M_CLR = LD_MISC;
constexpr int M_CCNT = LD_MISC + 4;
constexpr int M_SCALE = LD_MISC + 16;
constexpr int M_WMAX = LD_MISC + 64;
constexpr int M_SEL = LD_MISC + 128;
constexpr int M_STAMP = LD_MISC + 512;
constexpr int Z0 = 2064;

constexpr int LF_W = 0;
constexpr int LF_CAND = 16384;
constexpr int LF_Z = 32768;
constexpr int LF_H = LF_Z + 8 * HT_ROW;
constexpr int LF_RED = 57344;
constexpr int LF_ACT = 73728;
constexpr int ACT_ROW = 3088;

DEV float bf_lo(u32 v) { return __uint_as_float(v << 16); }
DEV float bf_hi(u32 v) { return __uint_as_float(v & 0xffff0000u); }
DEV float bf2f(u16 v) { return __uint_as_float((u32)v << 16); }
DEV u32 pk_bf(float a, float b) {
    f2 v = {a, b};
    return __builtin_bit_cast(u32, __builtin_convertvector(v, b2));
}
DEV h2 as_h2(u32 v) { return __builtin_bit_cast(h2, v); }
DEV float rbf(float a) { return bf_lo(pk_bf(a, a)); }
DEV u32 pk_h(float a, float b) {
    f2 v = {a, b};
    return __builtin_bit_cast(u32, __builtin_convertvector(v, h2));
}
template <int CTRL> DEV float dppf(float v) {
    return __int_as_float(__builtin_amdgcn_update_dpp(0, __float_as_int(v), CTRL, 0xf, 0xf, true));
}
template <int CTRL> DEV u32 dppu(u32 v) { return (u32)__builtin_amdgcn_update_dpp(0, (int)v, CTRL, 0xf, 0xf, true); }
DEV float wave_sum(float v) {
    v += dppf<0xB1>(v);
    v += dppf<0x4E>(v);
    v += dppf<0x141>(v);
    v += dppf<0x140>(v);
    auto r = __builtin_amdgcn_permlane16_swap(__float_as_uint(v), __float_as_uint(v), false, false);
    v = __uint_as_float(r[0]) + __uint_as_float(r[1]);
    auto q = __builtin_amdgcn_permlane32_swap(__float_as_uint(v), __float_as_uint(v), false, false);
    return __uint_as_float(q[0]) + __uint_as_float(q[1]);
}
DEV u32 umax(u32 a, u32 b) { return a > b ? a : b; }
DEV u32 wave_max(u32 v) {
    v = umax(v, dppu<0xB1>(v));
    v = umax(v, dppu<0x4E>(v));
    v = umax(v, dppu<0x141>(v));
    v = umax(v, dppu<0x140>(v));
    auto r = __builtin_amdgcn_permlane16_swap(v, v, false, false);
    v = umax(r[0], r[1]);
    auto q = __builtin_amdgcn_permlane32_swap(v, v, false, false);
    return umax(q[0], q[1]);
}

struct cf { float r, i; };
DEV cf cmul(cf a, cf b) { return {a.r * b.r - a.i * b.i, a.r * b.i + a.i * b.r}; }
DEV cf csq(cf a) { return {a.r * a.r - a.i * a.i, (a.r + a.r) * a.i}; }
DEV cf cexp_pos(float zr, float zi, float pos) {
    const float mag = __builtin_amdgcn_exp2f(zr * pos * 1.4426950408889634f);
    const float rev = __builtin_amdgcn_fractf(zi * pos * 0.15915494309189535f);
    return {mag * __builtin_amdgcn_cosf(rev), mag * __builtin_amdgcn_sinf(rev)};
}
DEV u32 c2h(cf a) { return pk_h(a.r, a.i); }
DEV u32 hcmul(u32 a_, u32 b_) {
    const h2 a = __builtin_bit_cast(h2, a_), b = __builtin_bit_cast(h2, b_);
    const h2 t = h2{a.x, a.x} * b;
    const h2 r = __builtin_elementwise_fma(h2{a.y, a.y}, h2{-b.y, b.x}, t);
    return __builtin_bit_cast(u32, r);
}

DEV void ln_stats(const u32 (&v)[5], float& m, float& s) {
    float sum = 0.f;
#pragma unroll
    for (int i = 0; i < 5; ++i) sum += bf_lo(v[i]) + bf_hi(v[i]);
    const float mean = wave_sum(sum) * (1.0f / D);
    float sq = 0.f;
#pragma unroll
    for (int i = 0; i < 5; ++i) {
        const float a = bf_lo(v[i]) - mean, b = bf_hi(v[i]) - mean;
        sq += a * a + b * b;
    }
    const float var = wave_sum(sq) * (1.0f / D);
    m = rbf(mean);
    s = rbf(__builtin_sqrtf(rbf(rbf(var) + 1e-5f)));
}
DEV float ln_apply(float x, float m, float s, float rs) {
    const float a = rbf(x - m);
    const float q = a * rs;
    const float e = __builtin_fmaf(-q, s, a);
    return rbf(__builtin_fmaf(e, rs, q));
}

DEV v4u ld_sc1(__amdgpu_buffer_rsrc_t rs, int off) { return __builtin_amdgcn_raw_buffer_load_b128(rs, off, 0, 16); }
DEV void st_sc1(__amdgpu_buffer_rsrc_t rs, int off, v4u v) { __builtin_amdgcn_raw_buffer_store_b128(v, rs, off, 0, 16); }
DEV void drain() { asm volatile("s_waitcnt vmcnt(0)" ::: "memory"); }
DEV void cbar() { asm volatile("" ::: "memory"); }

DEV u32 kidx(int lag) {
    const u32 ul = (u32)lag;
    const int sh = max(0, 24 - (int)__builtin_clz(ul | 1u));
    const u32 k = (ul + ((1u << sh) >> 1) - (sh ? 1u : 0u) + (sh ? ((ul >> sh) & 1u) : 0u)) >> sh;
    return ul < (u32)L ? 128u * sh + k : 660u;
}

template <int N0, int N1>
DEV void conv_load_frags(LAS const unsigned char* kc, int lane, h8 (&A)[29]) {
    const int q = lane & 15, g = lane >> 4;
    const int s0 = Z0 - q + 8 * g;
    const int sg = s0 & 3;
    LAS const unsigned char* p = kc + sg * KCS + 2 * (s0 - sg);
#pragma unroll
    for (int n = N0; n < N1; ++n) {
        const v2u lo = *(LAS const v2u*)(p - 64 * n);
        const v2u hi = *(LAS const v2u*)(p - 64 * n + 8);
        const v4u t = {lo.x, lo.y, hi.x, hi.y};
        A[n - N0] = __builtin_bit_cast(h8, t);
    }
}
template <int N0, int N1>
DEV void conv_run(const h8 (&A)[29], LAS const unsigned char* ub, f4 (&acc)[8]) {
#pragma unroll
    for (int nu = 8; nu >= -56; --nu) {
        bool any = false;
#pragma unroll
        for (int T = 0; T < 8; ++T) {
            const int n = nu + 8 * T;
            any |= (n >= N0 && n < N1);
        }
        if (!any) continue;
        const h8 B = *(LAS const h8*)(ub - 64 * nu);
#pragma unroll
        for (int T = 0; T < 8; ++T) {
            const int n = nu + 8 * T;
            if (n >= N0 && n < N1) acc[T] = __builtin_amdgcn_mfma_f32_16x16x32_f16(A[n - N0], B, acc[T], 0, 0, 0);
        }
    }
}
DEV constexpr int pslot(int job, int T) {
    return job == 0 ? T - 3 : (job == 1 ? (T <= 2 ? 4 + T : T + 1) : T + 5);
}

#define STAMP(i) do {} while (0)
#define STAMP_TID 0
}

__global__ void __launch_bounds__(NT, 3)
s4d_moe_fused(const u16* __restrict__ x, const u16* __restrict__ pLogA, const u16* __restrict__ pAim,
              const u16* __restrict__ pCr, const u16* __restrict__ pCi, const u16* __restrict__ pLogDt,
              const u16* __restrict__ pDs, const u16* __restrict__ pWr, const u16* __restrict__ pWg,
              const u16* __restrict__ pWu, const u16* __restrict__ pWd, u16* __restrict__ out,
              unsigned char* __restrict__ ws) {
    __shared__ __attribute__((aligned(16))) unsigned char smem_[160 * 1024];
    LAS unsigned char* sm = (LAS unsigned char*)smem_;
    const int tid = threadIdx.x, lane = tid & 63;
    const int w = __builtin_amdgcn_readfirstlane(tid >> 6);
    const int b = blockIdx.x, grp = b & 7, slot = b >> 3;
    const bool is_conv = slot < 27;
    const int d0 = 80 * grp + 3 * slot;
    const int nch = is_conv ? (slot == 26 ? 2 : 3) : 0;
    const int f = (slot - 27) * 8 + grp;
    const __amdgpu_buffer_rsrc_t rws = __builtin_amdgcn_make_buffer_rsrc((void*)ws, 0, WS_END, 0x00020000);

    STAMP(0);
    if (tid == 0) { *(LAS u32*)(sm + M_CLR) = 0; *(LAS u32*)(sm + M_CCNT) = 0; }

    u32 xrow[5], wr[5];
    const u32 tag = *(const u32*)(ws + WS_EPOCH) + 1u;
    if (w < 8) {
        const u32* xr = (const u32*)(x + (size_t)(8 * b + w) * D);
#pragma unroll
        for (int i = 0; i < 5; ++i) xrow[i] = xr[lane + 64 * i];
    }
    auto phase1 = [&]() {
        if (w < 8) {
#pragma unroll
            for (int i = 0; i < 5; ++i) wr[i] = ((const u32*)pWr)[lane + 64 * i];
        }
        if (w >= 9) {
            const int t = tid - 576;
            const v4u sv = {SENT, SENT, SENT, SENT};
#pragma unroll
            for (int i = 0; i < 4; ++i) {
                const int p = t + 192 * i;
                if (p < D) {
                    st_sc1(rws, WS_HX + b * (D * 16) + p * 16, sv);
                    st_sc1(rws, WS_HROW + b * (D * 16) + p * 16, sv);
                }
            }
            if (t < 4) st_sc1(rws, WS_G3 + b * 64 + t * 16, sv);
            if (!is_conv) {
                if (f < 24 && t >= 64 && t < 128)
                    st_sc1(rws, WS_ACT + ((t - 64) >> 3) * (F * 2) + f * 128 + (t & 7) * 16, sv);
                if (f < 8 && t >= 128 && t < 130) st_sc1(rws, WS_IDX + f * 256 + (t - 128) * 16, sv);
            }
        }
        if (w < 8) {
            float m, s;
            ln_stats(xrow, m, s);
            const float rs = __builtin_amdgcn_rcpf(s);
#pragma unroll
            for (int i = 0; i < 5; ++i)
                *(LAS u32*)(sm + LD_UT + w * HT_ROW + (lane + 64 * i) * 4) =
                    pk_h(ln_apply(bf_lo(xrow[i]), m, s, rs), ln_apply(bf_hi(xrow[i]), m, s, rs));
        }
        __syncthreads();
        STAMP(1);
        if (w >= 9) {
            const int t = tid - 576;
#pragma unroll
            for (int i = 0; i < 2; ++i) {
                const int cp = t + 192 * i;
                if (cp < 320) {
                    u32 v[8];
#pragma unroll
                    for (int k = 0; k < 8; ++k) v[k] = *(LAS const u32*)(sm + LD_UT + k * HT_ROW + cp * 4);
                    const v4u lo = {__builtin_amdgcn_perm(v[1], v[0], 0x05040100u), __builtin_amdgcn_perm(v[3], v[2], 0x05040100u),
                                    __builtin_amdgcn_perm(v[5], v[4], 0x05040100u), __builtin_amdgcn_perm(v[7], v[6], 0x05040100u)};
                    const v4u hi = {__builtin_amdgcn_perm(v[1], v[0], 0x07060302u), __builtin_amdgcn_perm(v[3], v[2], 0x07060302u),
                                    __builtin_amdgcn_perm(v[5], v[4], 0x07060302u), __builtin_amdgcn_perm(v[7], v[6], 0x07060302u)};
                    st_sc1(rws, WS_UX + b * (D * 16) + cp * 32, lo);
                    st_sc1(rws, WS_UX + b * (D * 16) + cp * 32 + 16, hi);
                }
            }
            drain();
            u32 old = 0;
            if (lane == 0) old = __hip_atomic_fetch_add((LAS u32*)(sm + M_CLR), 1u, __ATOMIC_RELAXED, __HIP_MEMORY_SCOPE_WORKGROUP);
            old = __builtin_amdgcn_readfirstlane(old);
            if (old == 2u && lane < 8) __builtin_amdgcn_raw_buffer_store_b32(tag, rws, WS_G1 + lane * 1024 + b * 4, 0, 16);
        }
    };

    v4u wf[20];
    if (is_conv) {
        float zr = 0.f, zi = 0.f, cr = 0.f, ci = 0.f, sabs = 0.f;
        u16 pla = 0, pai = 0, pcr[3] = {0, 0, 0}, pci[3] = {0, 0, 0}, pdt = 0;
        if (w < 9) {
            const int d = min(d0 + w / 3, D - 1), n = (w % 3) * 64 + lane;
            pla = pLogA[d * NS + n];
            pai = pAim[d * NS + n];
            pdt = pLogDt[d];
#pragma unroll
            for (int i = 0; i < 3; ++i) {
                const int nn = ((w % 3) + i) % 3 * 64 + lane;
                pcr[i] = pCr[d * NS + nn];
                pci[i] = pCi[d * NS + nn];
            }
        }
        __syncthreads();
        phase1();
        if (w < 9) {
            const int c = w / 3, n = (w % 3) * 64 + lane;
            const float dt = rbf(__expf(bf2f(pdt)));
            zr = -rbf(__expf(bf2f(pla))) * dt;
            zi = bf2f(pai) * dt;
            cr = bf2f(pcr[0]) * dt;
            ci = bf2f(pci[0]) * dt;
            sabs = __builtin_fabsf(cr) + __builtin_fabsf(ci) + (__builtin_fabsf(bf2f(pcr[1]) * dt) + __builtin_fabsf(bf2f(pci[1]) * dt)) +
                   (__builtin_fabsf(bf2f(pcr[2]) * dt) + __builtin_fabsf(bf2f(pci[2]) * dt));
            LAS u32* T = (LAS u32*)(sm + LD_TBL + c * TCH + n * 4);
            constexpr int RS = TROW / 4;
            {
                const cf B1 = cexp_pos(zr, zi, 1.f), H1 = cexp_pos(zr, zi, 128.f);
                const cf B2 = csq(B1), B3 = cmul(B2, B1), B4 = csq(B2), B5 = cmul(B4, B1), B6 = csq(B3), B7 = cmul(B4, B3);
                u32 hb[8];
                hb[0] = 0x00003C00u;
                hb[1] = c2h(B1); hb[2] = c2h(B2); hb[3] = c2h(B3); hb[4] = c2h(B4); hb[5] = c2h(B5); hb[6] = c2h(B6); hb[7] = c2h(B7);
                const cf H2 = csq(H1), H3 = cmul(H2, H1), H4 = csq(H2), H5 = cmul(H4, H1), H6 = csq(H3), H7 = cmul(H4, H3), H8 = csq(H4);
                const u32 h1 = c2h(H1), h2_ = c2h(H2), h3 = c2h(H3), h4 = c2h(H4), h5 = c2h(H5), h6 = c2h(H6), h7 = c2h(H7);
#pragma unroll
                for (int k = 0; k < 8; ++k) T[(16 + k) * RS] = hb[k];
                T[24 * RS] = h1;
#pragma unroll
                for (int k = 1; k < 8; ++k) T[(24 + k) * RS] = hcmul(h1, hb[k]);
                T[32 * RS] = h2_; T[33 * RS] = hcmul(h2_, hb[2]); T[34 * RS] = hcmul(h2_, hb[4]); T[35 * RS] = hcmul(h2_, hb[6]);
                T[36 * RS] = h3;  T[37 * RS] = hcmul(h3, hb[2]);  T[38 * RS] = hcmul(h3, hb[4]);  T[39 * RS] = hcmul(h3, hb[6]);
                T[40 * RS] = h4;  T[41 * RS] = hcmul(h4, hb[4]);
                T[42 * RS] = h5;  T[43 * RS] = hcmul(h5, hb[4]);
                T[44 * RS] = h6;  T[45 * RS] = hcmul(h6, hb[4]);
                T[46 * RS] = h7;  T[47 * RS] = hcmul(h7, hb[4]);
                T[48 * RS] = c2h(H8);
                T[49 * RS] = c2h(cmul(H8, H1));
                T[50 * RS] = c2h(csq(H5));
                T[51 * RS] = c2h(cmul(H8, H3));
                T[52 * RS] = c2h(csq(H6));
                T[53 * RS] = c2h(cmul(H8, H5));
                T[54 * RS] = c2h(csq(H7));
                T[55 * RS] = c2h(cmul(H8, H7));
                T[56 * RS] = c2h(csq(H8));
            }
            {
                const float tot = wave_sum(sabs) * 1.0001f;
                int e = 14 - __builtin_amdgcn_frexp_expf(tot);
                e = e > 100 ? 100 : (e < -100 ? -100 : e);
                if ((w % 3) == 0 && lane == 0) *(LAS int*)(sm + M_SCALE + 4 * c) = e;
            }
        }
        STAMP(2);
        __syncthreads();
        if (w < 9) {
            const int c = w / 3, n = (w % 3) * 64 + lane;
            const int e = *(LAS const int*)(sm + M_SCALE + 4 * c);
            const float sc = __builtin_ldexpf(1.f, e + 1);
            LAS u32* T = (LAS u32*)(sm + LD_TBL + c * TCH + n * 4);
            constexpr int RS = TROW / 4;
            const cf w8 = cexp_pos(zr, zi, 8.f);
            cf be = {cr * sc, ci * sc};
#pragma unroll
            for (int j = 0; j < 16; ++j) {
                T[j * RS] = pk_h(be.r, -be.i);
                if (j < 15) be = cmul(be, w8);
            }
            if ((w % 3) == 0 && lane < 16) *(LAS u16*)(sm + LD_KD + c * (KD_LEN * 2) + (656 + lane) * 2) = 0;
        }
        __syncthreads();
        STAMP(3);
        if (w < 9) {
            const int c = w / 3, tile = w % 3;
            const int j = lane & 15, g = lane >> 4;
            int row = 16 + 16 * tile + j;
            if (row > 56) row = 56;
            LAS const unsigned char* pa = sm + LD_TBL + c * TCH + row * TROW + g * 16;
            LAS const unsigned char* pb = sm + LD_TBL + c * TCH + j * TROW + g * 16;
            f4 acc = {0.f, 0.f, 0.f, 0.f};
#pragma unroll
            for (int t = 0; t < 12; ++t)
                acc = __builtin_amdgcn_mfma_f32_16x16x32_f16(*(LAS const h8*)(pa + 64 * t), *(LAS const h8*)(pb + 64 * t), acc, 0, 0, 0);
            const u32 p01 = pk_h(acc[0], acc[1]), p23 = pk_h(acc[2], acc[3]);
            LAS unsigned char* kd = sm + LD_KD + c * (KD_LEN * 2);
            if (tile == 0) {
                *(LAS v2u*)(kd + 2 * (128 * (g >> 1) + 4 * (g & 1) + 8 * j)) = v2u{p01, p23};
            } else if (tile == 1) {
                if (g < 2) {
                    *(LAS v2u*)(kd + 2 * (256 + 64 * g + 4 * j)) = v2u{p01, p23};
                } else {
                    *(LAS u32*)(kd + 2 * (384 + 64 * (g - 2) + 2 * j)) = p01;
                    *(LAS u32*)(kd + 2 * (384 + 64 * (g - 2) + 32 + 2 * j)) = p23;
                }
            } else {
                const int r0 = 4 * g;
                if (r0 <= 8) *(LAS u16*)(kd + 2 * (512 + 16 * r0 + j)) = (u16)p01;
                if (r0 + 3 <= 8) {
                    *(LAS u16*)(kd + 2 * (512 + 16 * (r0 + 1) + j)) = (u16)(p01 >> 16);
                    *(LAS u16*)(kd + 2 * (512 + 16 * (r0 + 2) + j)) = (u16)p23;
                    *(LAS u16*)(kd + 2 * (512 + 16 * (r0 + 3) + j)) = (u16)(p23 >> 16);
                }
            }
        }
        __syncthreads();
        STAMP(4);
        {
            auto expand = [&](int c, int a) {
                LAS const unsigned char* kd = sm + LD_KD + c * (KD_LEN * 2);
                u32 P[6];
#pragma unroll
                for (int k = 0; k < 6; ++k)
                    P[k] = (u32) * (LAS const u16*)(kd + 2 * kidx(Z0 - 8 * a - 2 * k)) |
                           ((u32) * (LAS const u16*)(kd + 2 * kidx(Z0 - 8 * a - 2 * k - 1)) << 16);
                LAS unsigned char* dst = sm + LD_KC + c * (4 * KCS) + a * 16;
                *(LAS v4u*)(dst) = v4u{P[0], P[1], P[2], P[3]};
                *(LAS v4u*)(dst + KCS) = v4u{__builtin_amdgcn_alignbit(P[1], P[0], 16), __builtin_amdgcn_alignbit(P[2], P[1], 16),
                                             __builtin_amdgcn_alignbit(P[3], P[2], 16), __builtin_amdgcn_alignbit(P[4], P[3], 16)};
                *(LAS v4u*)(dst + 2 * KCS) = v4u{P[1], P[2], P[3], P[4]};
                *(LAS v4u*)(dst + 3 * KCS) = v4u{__builtin_amdgcn_alignbit(P[2], P[1], 16), __builtin_amdgcn_alignbit(P[3], P[2], 16),
                                                 __builtin_amdgcn_alignbit(P[4], P[3], 16), __builtin_amdgcn_alignbit(P[5], P[4], 16)};
            };
            expand(tid >> 8, 1 + (tid & 255));
            if (tid >= 704 && tid < 728) { const int t = tid - 704; expand(t >> 3, (t & 7) == 0 ? 0 : 256 + (t & 7)); }
            if (tid < 102) {
                const int c = tid / 34, k = tid % 34;
                const int el = k < 32 ? 8 * k : 2304 + 8 * (k - 32);
                *(LAS v4u*)(sm + LD_UU + c * (ULEN * 2) + el * 2) = v4u{0, 0, 0, 0};
            }
        }
        __syncthreads();
        STAMP(5);
        int job = -1, jc = 0;
        if ((w & 3) == 0) { job = 2; jc = w >> 2; }
        else if (w < 8) { job = (w >> 2); jc = (w & 3) - 1; }
        h8 A[29];
        LAS const unsigned char* kc = sm + LD_KC + jc * (4 * KCS);
        if (job == 0) conv_load_frags<0, 16>(kc, lane, A);
        else if (job == 1) conv_load_frags<16, 36>(kc, lane, A);
        else if (job == 2) conv_load_frags<36, 65>(kc, lane, A);
        STAMP(6);
        {
            const int o = tid / 3, c = tid - 3 * o;
            u32 sp = 0;
            while (true) {
                cbar();
                const u32 fl = __builtin_amdgcn_raw_buffer_load_b32(rws, WS_G1 + (b & 7) * 1024 + o * 4, 0, 16);
                if (__all(fl == tag) || ++sp > SPIN_CAP) break;
            }
            cbar();
            STAMP(7);
            const v4u v = ld_sc1(rws, WS_UX + o * (D * 16) + min(d0 + c, D - 1) * 16);
            *(LAS v4u*)(sm + LD_UU + c * (ULEN * 2) + (256 + 8 * o) * 2) = v;
        }
        __syncthreads();
        STAMP(8);
        f4 acc[8];
#pragma unroll
        for (int T = 0; T < 8; ++T) acc[T] = f4{0.f, 0.f, 0.f, 0.f};
        {
            const int p = lane & 15, g = lane >> 4;
            LAS const unsigned char* ub = sm + LD_UU + jc * (ULEN * 2) + (256 + 16 * p + 8 * g) * 2;
            LAS unsigned char* part = sm + LD_PART + jc * (11 * 1024) + lane * 16;
            if (job == 0) {
                conv_run<0, 16>(A, ub, acc);
#pragma unroll
                for (int T = 3; T < 8; ++T) *(LAS f4*)(part + pslot(0, T) * 1024) = acc[T];
            } else if (job == 1) {
                conv_run<16, 36>(A, ub, acc);
                *(LAS f4*)(part + pslot(1, 1) * 1024) = acc[1];
                *(LAS f4*)(part + pslot(1, 2) * 1024) = acc[2];
                *(LAS f4*)(part + pslot(1, 6) * 1024) = acc[6];
                *(LAS f4*)(part + pslot(1, 7) * 1024) = acc[7];
            } else if (job == 2) {
                conv_run<36, 65>(A, ub, acc);
                *(LAS f4*)(part + pslot(2, 4) * 1024) = acc[4];
                *(LAS f4*)(part + pslot(2, 5) * 1024) = acc[5];
            }
        }
        STAMP(9);
        __syncthreads();
        STAMP(10);
        if (job >= 0 && jc < nch) {
            const int p = lane & 15, g = lane >> 4;
            const float isc = __builtin_ldexpf(1.f, -*(LAS const int*)(sm + M_SCALE + 4 * jc));
            const float dsk = bf2f(pDs[d0 + jc]);
            LAS const unsigned char* part = sm + LD_PART + jc * (11 * 1024) + lane * 16;
            auto fin = [&](int T, f4 y) {
                const int tok = 256 * T + 16 * p + 4 * g;
                const v2u uu = *(LAS const v2u*)(sm + LD_UU + jc * (ULEN * 2) + (256 + tok) * 2);
                const h2 u01 = as_h2(uu.x), u23 = as_h2(uu.y);
                const float uf[4] = {(float)u01.x, (float)u01.y, (float)u23.x, (float)u23.y};
                float h[4];
#pragma unroll
                for (int r = 0; r < 4; ++r) h[r] = rbf(y[r] * isc) + rbf(uf[r] * dsk);
                *(LAS v2u*)(sm + LD_HB + jc * (L * 2) + tok * 2) = v2u{pk_bf(h[0], h[1]), pk_bf(h[2], h[3])};
            };
            auto P = [&](int j, int T) { return *(LAS const f4*)(part + pslot(j, T) * 1024); };
            if (job == 0) {
                fin(0, acc[0]);
                fin(1, acc[1] + P(1, 1));
                fin(2, acc[2] + P(1, 2));
            } else if (job == 1) {
                fin(3, acc[3] + P(0, 3));
                fin(4, acc[4] + P(0, 4) + P(2, 4));
                fin(5, acc[5] + P(0, 5) + P(2, 5));
            } else {
                fin(6, acc[6] + P(0, 6) + P(1, 6));
                fin(7, acc[7] + P(0, 7) + P(1, 7));
            }
        }
        __syncthreads();
        STAMP(11);
        {
            const int o = tid / 3, c = tid - 3 * o;
            if (c < nch) {
                const v4u v = *(LAS const v4u*)(sm + LD_HB + c * (L * 2) + o * 16);
                st_sc1(rws, WS_HX + o * (D * 16) + (d0 + c) * 16, v);
            }
            drain();
        }
    } else {
        v4u rw[16];
        auto load_weights = [&]() {
            if (f < 24) {
#pragma unroll
                for (int i = 0; i < 7; ++i) {
                    const int p = min(tid + NT * i, 5119);
                    rw[i] = *(const v4u*)(pWg + (size_t)(p >> 3) * F + 64 * f + 8 * (p & 7));
                    rw[8 + i] = *(const v4u*)(pWu + (size_t)(p >> 3) * F + 64 * f + 8 * (p & 7));
                }
            } else if (f < 34) {
#pragma unroll
                for (int i = 0; i < 16; ++i) {
                    const int p = tid + NT * i;
                    rw[i] = *(const v4u*)(pWd + (size_t)(p >> 3) * D + 64 * (f - 24) + 8 * (p & 7));
                }
            }
        };
        if (w < 9) load_weights();
        __syncthreads();
        phase1();
        if (w >= 9) load_weights();
        const int g = lane >> 4, q = (lane & 15) >> 2, pp = lane & 3;
        if (f < 24) {
#pragma unroll
            for (int rnd = 0; rnd < 2; ++rnd) {
#pragma unroll
                for (int i = 0; i < 7; ++i) {
                    const int p = tid + NT * i;
                    if (p < 5120) *(LAS v4u*)(sm + LF_W + p * 16) = rw[8 * rnd + i];
                }
                __syncthreads();
                if (w < 8 && (w >> 2) == rnd) {
                    LAS const unsigned char* base = sm + LF_W + (w & 3) * 32 + pp * 8;
#pragma unroll
                    for (int s = 0; s < 20; ++s) {
                        const int k0 = 32 * s + 8 * g + q;
                        const s4 a = __builtin_amdgcn_ds_read_tr16_b64_v4i16((LAS s4*)(base + k0 * 128));
                        const s4 c = __builtin_amdgcn_ds_read_tr16_b64_v4i16((LAS s4*)(base + (k0 + 4) * 128));
                        const v2u a2 = __builtin_bit_cast(v2u, a), c2 = __builtin_bit_cast(v2u, c);
                        wf[s] = v4u{a2.x, a2.y, c2.x, c2.y};
                    }
                }
                __syncthreads();
            }
        } else if (f < 34) {
#pragma unroll
            for (int rnd = 0; rnd < 2; ++rnd) {
#pragma unroll
                for (int i = 0; i < 8; ++i) *(LAS v4u*)(sm + LF_W + (tid + NT * i) * 16) = rw[8 * rnd + i];
                __syncthreads();
                LAS const unsigned char* base = sm + LF_W + (w & 3) * 32 + pp * 8;
#pragma unroll
                for (int s = 0; s < 16; ++s) {
                    const int ks = 16 * (w >> 2) + s;
                    if ((ks >= 24) == (rnd == 1)) {
                        const int k0 = 32 * (ks - 24 * rnd) + 8 * g + q;
                        const s4 a = __builtin_amdgcn_ds_read_tr16_b64_v4i16((LAS s4*)(base + k0 * 128));
                        const s4 c = __builtin_amdgcn_ds_read_tr16_b64_v4i16((LAS s4*)(base + (k0 + 4) * 128));
                        const v2u a2 = __builtin_bit_cast(v2u, a), c2 = __builtin_bit_cast(v2u, c);
                        wf[s] = v4u{a2.x, a2.y, c2.x, c2.y};
                    }
                }
                __syncthreads();
            }
        }
        if (w == 0) {
            u32 sp = 0;
            while (true) {
                cbar();
                const u32 v = __builtin_amdgcn_raw_buffer_load_b32(rws, WS_HX + b * (D * 16), 0, 16);
                if (v != SENT || ++sp > SPIN_CAP) break;
                __builtin_amdgcn_s_sleep(4);
            }
        }
    }
    __syncthreads();
    STAMP(12);

    if (tid < D) {
        v4u v;
        u32 sp = 0;
        while (true) {
            cbar();
            v = ld_sc1(rws, WS_HX + b * (D * 16) + tid * 16);
            const bool ok = (v.x != SENT) & (v.y != SENT) & (v.z != SENT) & (v.w != SENT);
            if (__all(ok) || ++sp > SPIN_CAP) break;
        }
        STAMP(13);
        const bool odd = tid & 1;
        const u32 s0 = odd ? v.x : v.z, s1 = odd ? v.y : v.w;
        const u32 r0 = dppu<0xB1>(s0), r1 = dppu<0xB1>(s1);
        const u32 k0 = odd ? v.z : v.x, k1 = odd ? v.w : v.y;
        const u32 lo0 = odd ? r0 : k0, hi0 = odd ? k0 : r0, lo1 = odd ? r1 : k1, hi1 = odd ? k1 : r1;
        LAS u32* dst = (LAS u32*)(sm + LD_HT + (odd ? 4 * HT_ROW : 0) + (tid >> 1) * 4);
        dst[0 * (HT_ROW / 4)] = __builtin_amdgcn_perm(hi0, lo0, 0x05040100u);
        dst[1 * (HT_ROW / 4)] = __builtin_amdgcn_perm(hi0, lo0, 0x07060302u);
        dst[2 * (HT_ROW / 4)] = __builtin_amdgcn_perm(hi1, lo1, 0x05040100u);
        dst[3 * (HT_ROW / 4)] = __builtin_amdgcn_perm(hi1, lo1, 0x07060302u);
    }
    __syncthreads();
    u32 orow[5];
    const int mytok = 8 * b + w;
    if (w < 8) {
        u32 hv[5];
#pragma unroll
        for (int i = 0; i < 5; ++i) {
            const u32 a = *(LAS const u32*)(sm + LD_HT + w * HT_ROW + (lane + 64 * i) * 4);
            hv[i] = pk_bf(bf_lo(xrow[i]) + bf_lo(a), bf_hi(xrow[i]) + bf_hi(a));
        }
        float m, s;
        ln_stats(hv, m, s);
        const float rs = __builtin_amdgcn_rcpf(s);
        float racc = 0.f;
#pragma unroll
        for (int i = 0; i < 5; ++i) {
            const float h0 = bf_lo(hv[i]), h1 = bf_hi(hv[i]);
            const float z0 = ln_apply(h0, m, s, rs), z1 = ln_apply(h1, m, s, rs);
            racc += z0 * bf_lo(wr[i]) + z1 * bf_hi(wr[i]);
            orow[i] = pk_bf(h0 + z0, h1 + z1);
        }
        const float r = wave_sum(racc);
        if (lane == 0) {
            v2u g;
            g.x = (__float_as_uint(m) >> 16) | (__float_as_uint(s) & 0xffff0000u);
            g.y = pk_bf(r, 0.f) & 0xffffu;
            __builtin_amdgcn_raw_buffer_store_b64(g, rws, WS_G3 + mytok * 8, 0, 16);
        }
        STAMP(14);
#pragma unroll
        for (int i = 0; i < 5; ++i)
            __builtin_amdgcn_raw_buffer_store_b32(hv[i], rws, WS_HROW + mytok * (D * 2) + (lane + 64 * i) * 4, 0, 16);
    }
    STAMP(15);

    if (!is_conv && f < 34) {
        u32 key[4], msv[4];
        if (w < 8) {
            __builtin_amdgcn_s_sleep(8);
            v4u g0, g1;
            u32 sp = 0;
            while (true) {
                cbar();
                g0 = ld_sc1(rws, WS_G3 + tid * 32);
                g1 = ld_sc1(rws, WS_G3 + tid * 32 + 16);
                const bool ok = (g0.y != SENT) & (g0.w != SENT) & (g1.y != SENT) & (g1.w != SENT);
                if (__all(ok) || ++sp > SPIN_CAP) break;
            }
            STAMP(16);
            const u32 rb[4] = {g0.y, g0.w, g1.y, g1.w};
            msv[0] = g0.x; msv[1] = g0.z; msv[2] = g1.x; msv[3] = g1.z;
#pragma unroll
            for (int j = 0; j < 4; ++j) {
                const u32 o = (rb[j] & 0x8000u) ? (~rb[j] & 0xffffu) : (rb[j] | 0x8000u);
                key[j] = (o << 11) | (u32)(2047 - (4 * tid + j));
            }
            const u32 mx = wave_max(umax(umax(key[0], key[1]), umax(key[2], key[3])));
            if (lane == 0) *(LAS u32*)(sm + M_WMAX + 4 * w) = mx;
        }
        __syncthreads();
        if (w < 8) {
            const v4u a = *(LAS const v4u*)(sm + M_WMAX), c = *(LAS const v4u*)(sm + M_WMAX + 16);
            const u32 thr = min(min(min(a.x, a.y), min(a.z, a.w)), min(min(c.x, c.y), min(c.z, c.w)));
            u64 bal[4];
            int tot = 0;
#pragma unroll
            for (int j = 0; j < 4; ++j) { bal[j] = __ballot(key[j] >= thr); tot += __builtin_popcountll(bal[j]); }
            u32 base = 0;
            if (lane == 0) base = __hip_atomic_fetch_add((LAS u32*)(sm + M_CCNT), (u32)tot, __ATOMIC_RELAXED, __HIP_MEMORY_SCOPE_WORKGROUP);
            base = __builtin_amdgcn_readfirstlane(base);
#pragma unroll
            for (int j = 0; j < 4; ++j) {
                const u32 pos = base + __builtin_amdgcn_mbcnt_hi((u32)(bal[j] >> 32), __builtin_amdgcn_mbcnt_lo((u32)bal[j], 0));
                if (key[j] >= thr) {
                    *(LAS u32*)(sm + LF_CAND + pos * 4) = key[j];
                    *(LAS u32*)(sm + LF_CAND + 8192 + pos * 4) = msv[j];
                }
                base += __builtin_popcountll(bal[j]);
            }
        }
        __syncthreads();
        {
            const u32 cnt = *(LAS const u32*)(sm + M_CCNT);
            for (u32 i0 = 64 * w; i0 < cnt; i0 += NT) {
                const u32 i = i0 + lane;
                const u32 me = i < cnt ? *(LAS const u32*)(sm + LF_CAND + i * 4) : 0u;
                u32 rank = 0;
                for (u32 j0 = 0; j0 < cnt; j0 += 64) {
                    const u32 ot = (j0 + lane) < cnt ? *(LAS const u32*)(sm + LF_CAND + (j0 + lane) * 4) : 0u;
                    const u32 nj = min(64u, cnt - j0);
                    for (u32 j = 0; j < nj; ++j) rank += (u32)__builtin_amdgcn_readlane(ot, j) > me;
                }
                if (i < cnt && rank < 8) {
                    const u32 rbits = me >> 11;
                    const u32 rb = (rbits & 0x8000u) ? (rbits & 0x7fffu) : (~rbits & 0xffffu);
                    *(LAS v4u*)(sm + M_SEL + rank * 16) =
                        v4u{2047u - (me & 2047u), *(LAS const u32*)(sm + LF_CAND + 8192 + i * 4), rb, 0u};
                }
            }
        }
        __syncthreads();
        STAMP(17);
        if (f < 8 && tid < 2) {
            const v4u s0 = *(LAS const v4u*)(sm + M_SEL + tid * 64), s1 = *(LAS const v4u*)(sm + M_SEL + tid * 64 + 16),
                      s2 = *(LAS const v4u*)(sm + M_SEL + tid * 64 + 32), s3 = *(LAS const v4u*)(sm + M_SEL + tid * 64 + 48);
            st_sc1(rws, WS_IDX + f * 256 + tid * 16, v4u{s0.x, s1.x, s2.x, s3.x});
        }
        if (tid < D) {
            const int k = tid / 80, ch = tid % 80;
            const v4u sel = *(LAS const v4u*)(sm + M_SEL + k * 16);
            v4u hv;
            u32 sp = 0;
            while (true) {
                cbar();
                hv = ld_sc1(rws, WS_HROW + (int)(sel.x & (L - 1)) * (D * 2) + ch * 16);
                const bool ok = (hv.x != SENT) & (hv.y != SENT) & (hv.z != SENT) & (hv.w != SENT);
                if (__all(ok) || ++sp > SPIN_CAP) break;
            }
            const float m = bf_lo(sel.y), s = bf_hi(sel.y), rs = __builtin_amdgcn_rcpf(s);
            const u32 hh[4] = {hv.x, hv.y, hv.z, hv.w};
            u32 zz[4];
#pragma unroll
            for (int i = 0; i < 4; ++i) zz[i] = pk_bf(ln_apply(bf_lo(hh[i]), m, s, rs), ln_apply(bf_hi(hh[i]), m, s, rs));
            *(LAS v4u*)(sm + LF_Z + k * HT_ROW + ch * 16) = v4u{zz[0], zz[1], zz[2], zz[3]};
            *(LAS v4u*)(sm + LF_H + k * HT_ROW + ch * 16) = hv;
        }
        __syncthreads();
        STAMP(18);
        const int row = lane & 15, g = lane >> 4;
        if (f < 24) {
            f4 acc = {0.f, 0.f, 0.f, 0.f};
            if (w < 8) {
#pragma unroll
                for (int s = 0; s < 20; ++s) {
                    v4u a = {0, 0, 0, 0};
                    if (row < 8) a = *(LAS const v4u*)(sm + LF_Z + row * HT_ROW + (32 * s + 8 * g) * 2);
                    acc = __builtin_amdgcn_mfma_f32_16x16x32_bf16(__builtin_bit_cast(b8, a), __builtin_bit_cast(b8, wf[s]), acc, 0, 0, 0);
                }
                if (w >= 4) *(LAS f4*)(sm + LF_RED + (w - 4) * 1024 + lane * 16) = acc;
            }
            __syncthreads();
            if (w < 4) {
                const f4 up = *(LAS const f4*)(sm + LF_RED + w * 1024 + lane * 16);
                float a[4];
#pragma unroll
                for (int r = 0; r < 4; ++r) {
                    const float gb = rbf(acc[r]);
                    const float sg = rbf(gb / (1.f + __expf(-gb)));
                    a[r] = sg * rbf(up[r]);
                }
                const u32 p01 = pk_bf(a[0], a[1]), p23 = pk_bf(a[2], a[3]);
                const bool odd = lane & 1;
                const u32 rcv = dppu<0xB1>(odd ? p01 : p23), own = odd ? p23 : p01;
                const u32 lo = odd ? rcv : own, hi = odd ? own : rcv;
                if (g < 2) {
                    const int tok = 4 * g + (odd ? 2 : 0);
                    const int off = WS_ACT + tok * (F * 2) + (64 * f + 16 * w + (row & 14)) * 2;
                    __builtin_amdgcn_raw_buffer_store_b32(__builtin_amdgcn_perm(hi, lo, 0x05040100u), rws, off, 0, 16);
                    __builtin_amdgcn_raw_buffer_store_b32(__builtin_amdgcn_perm(hi, lo, 0x07060302u), rws, off + F * 2, 0, 16);
                }
            }
            STAMP(19);
        } else {
            {
                v4u a0, a1;
                u32 sp = 0;
                __builtin_amdgcn_s_sleep(16);
                while (true) {
                    cbar();
                    a0 = ld_sc1(rws, WS_ACT + tid * 16);
                    a1 = ld_sc1(rws, WS_ACT + (tid + NT) * 16);
                    const bool ok = (a0.x != SENT) & (a0.y != SENT) & (a0.z != SENT) & (a0.w != SENT) & (a1.x != SENT) &
                                    (a1.y != SENT) & (a1.z != SENT) & (a1.w != SENT);
                    if (__all(ok) || ++sp > SPIN_CAP) break;
                }
                *(LAS v4u*)(sm + LF_ACT + (tid / 192) * ACT_ROW + (tid % 192) * 16) = a0;
                *(LAS v4u*)(sm + LF_ACT + ((tid + NT) / 192) * ACT_ROW + ((tid + NT) % 192) * 16) = a1;
            }
            __syncthreads();
            STAMP(20);
            f4 acc = {0.f, 0.f, 0.f, 0.f};
#pragma unroll
            for (int s = 0; s < 16; ++s) {
                v4u a = {0, 0, 0, 0};
                if (row < 8) a = *(LAS const v4u*)(sm + LF_ACT + row * ACT_ROW + (32 * (16 * (w >> 2) + s) + 8 * g) * 2);
                acc = __builtin_amdgcn_mfma_f32_16x16x32_bf16(__builtin_bit_cast(b8, a), __builtin_bit_cast(b8, wf[s]), acc, 0, 0, 0);
            }
            *(LAS f4*)(sm + LF_RED + w * 1024 + lane * 16) = acc;
            __syncthreads();
            if (tid < 512) {
                const int tok = tid >> 6, col = tid & 63, tile = col >> 4;
                const int off = ((tok >> 2) * 16 + (col & 15)) * 16 + (tok & 3) * 4;
                const float y = *(LAS const float*)(sm + LF_RED + tile * 1024 + off) + *(LAS const float*)(sm + LF_RED + (4 + tile) * 1024 + off) +
                                *(LAS const float*)(sm + LF_RED + (8 + tile) * 1024 + off);
                const v4u sel = *(LAS const v4u*)(sm + M_SEL + tok * 16);
                const float r = bf2f((u16)sel.z);
                const float sg = rbf(1.f / (1.f + __expf(-r)));
                const float upd = rbf(rbf(y) * sg);
                const int d = 64 * (f - 24) + col;
                const float z = bf2f(*(LAS const u16*)(sm + LF_Z + tok * HT_ROW + d * 2));
                const float h = bf2f(*(LAS const u16*)(sm + LF_H + tok * HT_ROW + d * 2));
                out[(size_t)(sel.x & (L - 1)) * D + d] = (u16)pk_bf(h + rbf(z + upd), 0.f);
            }
        }
    }

    STAMP(21);
    if (w < 8) {
        v4u i0, i1;
        u32 sp = 0;
        while (true) {
            cbar();
            i0 = ld_sc1(rws, WS_IDX + (b & 7) * 256);
            i1 = ld_sc1(rws, WS_IDX + (b & 7) * 256 + 16);
            const bool ok = (i0.x != SENT) & (i0.y != SENT) & (i0.z != SENT) & (i0.w != SENT) & (i1.x != SENT) & (i1.y != SENT) &
                            (i1.z != SENT) & (i1.w != SENT);
            if (__all(ok) || ++sp > SPIN_CAP) break;
            __builtin_amdgcn_s_sleep(16);
        }
        const u32 t = (u32)mytok;
        const bool routed = (i0.x == t) | (i0.y == t) | (i0.z == t) | (i0.w == t) | (i1.x == t) | (i1.y == t) | (i1.z == t) | (i1.w == t);
        if (!routed) {
            u32* o = (u32*)(out + (size_t)mytok * D);
#pragma unroll
            for (int i = 0; i < 5; ++i) o[lane + 64 * i] = orow[i];
        }
        if (b == 0 && tid == 0) *(u32*)(ws + WS_EPOCH) = tag;
    }
}

extern "C" void kernel_launch(void* const* d_in, const int* in_sizes, int n_in, void* d_out, int out_size, void* d_ws,
                              size_t ws_size, hipStream_t stream) {
    (void)in_sizes; (void)n_in; (void)out_size; (void)ws_size;
    s4d_moe_fused<<<256, NT, 0, stream>>>((const u16*)d_in[0], (const u16*)d_in[1], (const u16*)d_in[2], (const u16*)d_in[3],
                                          (const u16*)d_in[4], (const u16*)d_in[5], (const u16*)d_in[6],
                                          (const u16*)d_in[8], (const u16*)d_in[9], (const u16*)d_in[10],
                                          (const u16*)d_in[11], (u16*)d_out, (unsigned char*)d_ws);
}
```

```cpp
#include <hip/hip_runtime.h>
#include <stdint.h>

typedef unsigned int u32;
typedef unsigned long long u64;
typedef unsigned short u16;
typedef u32 v4u __attribute__((ext_vector_type(4)));
typedef u32 v2u __attribute__((ext_vector_type(2)));
typedef float f4 __attribute__((ext_vector_type(4)));
typedef float f2 __attribute__((ext_vector_type(2)));
typedef _Float16 h2 __attribute__((ext_vector_type(2)));
typedef _Float16 h8 __attribute__((ext_vector_type(8)));
typedef __bf16 b2 __attribute__((ext_vector_type(2)));
typedef __bf16 b8 __attribute__((ext_vector_type(8)));
typedef short s4 __attribute__((ext_vector_type(4)));
#define LAS __attribute__((address_space(3)))
#define DEV __device__ __forceinline__

namespace {

constexpr int L = 2048, D = 640, NS = 192, F = 1536;
constexpr int NT = 768;
constexpr u32 SENT = 0xFFFFFFFFu;
constexpr u32 SPIN_CAP = 1u << 16;
#define DEP_L 4
#define DEP_M 4
#define DEP_H 4
#define E1_DELAY 12

constexpr int WS_EPOCH = 0;
constexpr int WS_G1 = 4096;
constexpr int WS_G3 = WS_G1 + 8192;
constexpr int WS_IDX = WS_G3 + 16384;
constexpr int WS_ACT = WS_IDX + 2048;
constexpr int WS_UX = 65536;
constexpr int WS_HX = WS_UX + L * D * 2;
constexpr int WS_HROW = WS_HX + L * D * 2;
constexpr int WS_END = WS_HROW + L * D * 2;
constexpr int WS_DBG = 16 << 20;

constexpr int TROW = 800;
constexpr int TCH = 57 * TROW;
constexpr int LD_TBL = 0;
constexpr int KD_LEN = 672;
constexpr int LD_KD = 136800;
constexpr int LD_UT = LD_KD + 3 * KD_LEN * 2;
constexpr int LD_MISC = 153600;
constexpr int KCS = 4416;
constexpr int LD_KC = 0;
constexpr int ULEN = 2320;
constexpr int PT_G = 320;
constexpr int PT = 4 * PT_G;
constexpr int PT_CH = 19 * PT + 48;
constexpr int LD_PART = 0;
constexpr int LD_UU = 99328;
constexpr int LD_XR = 114688;
constexpr int LD_HT = 0;
constexpr int HT_ROW = 648 * 2;
constexpr int M_CLR = LD_MISC;
constexpr int M_CCNT = LD_MISC + 4;
constexpr int M_SCALE = LD_MISC + 16;
constexpr int M_WMAX = LD_MISC + 64;
constexpr int M_SEL = LD_MISC + 128;
constexpr int M_STAMP = LD_MISC + 512;
constexpr int Z0 = 2064;

constexpr int LF_W = 0;
constexpr int LF_CAND = 16384;
constexpr int LF_Z = 32768;
constexpr int LF_H = LF_Z + 8 * HT_ROW;
constexpr int LF_RED = 57344;
constexpr int LF_ACT = 73728;
constexpr int ACT_ROW = 3088;

DEV float bf_lo(u32 v) { return __uint_as_float(v << 16); }
DEV float bf_hi(u32 v) { return __uint_as_float(v & 0xffff0000u); }
DEV float bf2f(u16 v) { return __uint_as_float((u32)v << 16); }
DEV u32 pk_bf(float a, float b) {
    f2 v = {a, b};
    return __builtin_bit_cast(u32, __builtin_convertvector(v, b2));
}
DEV h2 as_h2(u32 v) { return __builtin_bit_cast(h2, v); }
DEV float rbf(float a) { return bf_lo(pk_bf(a, a)); }
DEV u32 pk_h(float a, float b) {
    f2 v = {a, b};
    return __builtin_bit_cast(u32, __builtin_convertvector(v, h2));
}
template <int CTRL> DEV float dppf(float v) {
    return __int_as_float(__builtin_amdgcn_update_dpp(0, __float_as_int(v), CTRL, 0xf, 0xf, true));
}
template <int CTRL> DEV u32 dppu(u32 v) { return (u32)__builtin_amdgcn_update_dpp(0, (int)v, CTRL, 0xf, 0xf, true); }
DEV float wave_sum(float v) {
    v += dppf<0xB1>(v);
    v += dppf<0x4E>(v);
    v += dppf<0x141>(v);
    v += dppf<0x140>(v);
    auto r = __builtin_amdgcn_permlane16_swap(__float_as_uint(v), __float_as_uint(v), false, false);
    v = __uint_as_float(r[0]) + __uint_as_float(r[1]);
    auto q = __builtin_amdgcn_permlane32_swap(__float_as_uint(v), __float_as_uint(v), false, false);
    return __uint_as_float(q[0]) + __uint_as_float(q[1]);
}
DEV u32 umax(u32 a, u32 b) { return a > b ? a : b; }
DEV u32 wave_max(u32 v) {
    v = umax(v, dppu<0xB1>(v));
    v = umax(v, dppu<0x4E>(v));
    v = umax(v, dppu<0x141>(v));
    v = umax(v, dppu<0x140>(v));
    auto r = __builtin_amdgcn_permlane16_swap(v, v, false, false);
    v = umax(r[0], r[1]);
    auto q = __builtin_amdgcn_permlane32_swap(v, v, false, false);
    return umax(q[0], q[1]);
}

typedef f2 cf;
DEV cf cmul(cf a, cf b) { return __builtin_elementwise_fma(f2{a.y, a.y}, f2{-b.y, b.x}, f2{a.x, a.x} * b); }
DEV cf csq(cf a) { return cmul(a, a); }
DEV cf cexp_pos(float zr, float zi, float pos) {
    const float mag = __builtin_amdgcn_exp2f(zr * pos * 1.4426950408889634f);
    const float rev = __builtin_amdgcn_fractf(zi * pos * 0.15915494309189535f);
    return f2{mag * __builtin_amdgcn_cosf(rev), mag * __builtin_amdgcn_sinf(rev)};
}
DEV u32 c2h(cf a) { return pk_h(a.x, a.y); }
DEV u32 hcmul(u32 a_, u32 b_) {
    const h2 a = __builtin_bit_cast(h2, a_), b = __builtin_bit_cast(h2, b_);
    const h2 t = h2{a.x, a.x} * b;
    const h2 r = __builtin_elementwise_fma(h2{a.y, a.y}, h2{-b.y, b.x}, t);
    return __builtin_bit_cast(u32, r);
}

DEV void wave_sum2(float& a, float& b) {
    a += dppf<0xB1>(a);  b += dppf<0xB1>(b);
    a += dppf<0x4E>(a);  b += dppf<0x4E>(b);
    a += dppf<0x141>(a); b += dppf<0x141>(b);
    a += dppf<0x140>(a); b += dppf<0x140>(b);
    auto r = __builtin_amdgcn_permlane16_swap(__float_as_uint(a), __float_as_uint(a), false, false);
    auto t = __builtin_amdgcn_permlane16_swap(__float_as_uint(b), __float_as_uint(b), false, false);
    a = __uint_as_float(r[0]) + __uint_as_float(r[1]);
    b = __uint_as_float(t[0]) + __uint_as_float(t[1]);
    auto q = __builtin_amdgcn_permlane32_swap(__float_as_uint(a), __float_as_uint(a), false, false);
    auto u = __builtin_amdgcn_permlane32_swap(__float_as_uint(b), __float_as_uint(b), false, false);
    a = __uint_as_float(q[0]) + __uint_as_float(q[1]);
    b = __uint_as_float(u[0]) + __uint_as_float(u[1]);
}
DEV void ln_stats(const u32 (&v)[5], float& m, float& s, float& rs) {
    const float c = bf_lo(__builtin_amdgcn_readfirstlane(v[0]));
    float s1 = 0.f, s2 = 0.f;
#pragma unroll
    for (int i = 0; i < 5; ++i) {
        const float a = bf_lo(v[i]) - c, b = bf_hi(v[i]) - c;
        s1 += a + b;
        s2 = __builtin_fmaf(a, a, __builtin_fmaf(b, b, s2));
    }
    wave_sum2(s1, s2);
    const float dm = s1 * (1.0f / D);
    const float var = __builtin_fmaxf(__builtin_fmaf(-dm, dm, s2 * (1.0f / D)), 0.f);
    m = rbf(c + dm);
    const float v2 = rbf(rbf(var) + 1e-5f);
    const float q0 = __builtin_amdgcn_sqrtf(v2);
    const float q1 = __builtin_fmaf(__builtin_fmaf(-q0, q0, v2), 0.5f * __builtin_amdgcn_rcpf(q0), q0);
    s = rbf(q1);
    rs = __builtin_amdgcn_rcpf(s);
}
DEV u32 ln_apply2(float x0, float x1, float m, float s, float rs) {
    const u32 a = pk_bf(x0 - m, x1 - m);
    const float a0 = bf_lo(a), a1 = bf_hi(a);
    const float q0 = a0 * rs, q1 = a1 * rs;
    return pk_bf(__builtin_fmaf(__builtin_fmaf(-q0, s, a0), rs, q0), __builtin_fmaf(__builtin_fmaf(-q1, s, a1), rs, q1));
}

DEV v4u ld_sc1(__amdgpu_buffer_rsrc_t rs, int off) { return __builtin_amdgcn_raw_buffer_load_b128(rs, off, 0, 16); }
DEV void st_sc1(__amdgpu_buffer_rsrc_t rs, int off, v4u v) { __builtin_amdgcn_raw_buffer_store_b128(v, rs, off, 0, 16); }
DEV void drain() { asm volatile("s_waitcnt vmcnt(0)" ::: "memory"); }
DEV void cbar() { asm volatile("" ::: "memory"); }

constexpr u32 kidx(int lag) {
    if (lag < 0 || lag >= L) return 660u;
    const int sh = lag < 256 ? 0 : (lag < 512 ? 1 : (lag < 1024 ? 2 : 3));
    const int k = sh ? ((lag + (1 << (sh - 1)) - 1 + ((lag >> sh) & 1)) >> sh) : lag;
    return (u32)(128 * sh + k);
}
struct GatherTab { u32 v[264][8]; };
constexpr GatherTab make_gather_tab() {
    GatherTab t{};
    for (int a = 0; a < 264; ++a)
        for (int k = 0; k < 6; ++k) t.v[a][k] = (kidx(Z0 - 8 * a - 2 * k) * 2) | (kidx(Z0 - 8 * a - 2 * k - 1) * 2 << 16);
    return t;
}

template <int N0, int N1>
DEV void conv_load_frags(u32 kc, LAS const unsigned char* sm, int lane, h8 (&A)[29]) {
    const int q = lane & 15, g = lane >> 4;
    const int s0 = Z0 - q + 8 * g;
    const int sg = s0 & 3;
    u32 pl = kc + sg * KCS + 2 * (s0 - sg) - 64 * (N1 - 1);
    asm volatile("" : "+v"(pl));
#pragma unroll
    for (int n = N0; n < N1; ++n) {
        const v2u lo = *(LAS const v2u*)(sm + pl + 64 * (N1 - 1 - n));
        asm volatile("");
        const v2u hi = *(LAS const v2u*)(sm + pl + 64 * (N1 - 1 - n) + 8);
        asm volatile("");
        const v4u t = {lo.x, lo.y, hi.x, hi.y};
        A[n - N0] = __builtin_bit_cast(h8, t);
    }
}
template <int N0, int N1, int DEPTH>
DEV void conv_run(const h8 (&A)[29], LAS const unsigned char* ub_lo, f4 (&acc)[8]) {
    constexpr int NU_HI = N1 - 1 < 8 ? N1 - 1 : 8;
    constexpr int NU_LO = N0 - 56;
    constexpr int STEPS = NU_HI - NU_LO + 1;
    h8 B[DEPTH];
    constexpr int T0 = N0 <= 8 ? 0 : (N0 - 8 + 7) / 8;
#pragma unroll
    for (int T = T0; T < 8; ++T) asm volatile("" : "+v"(acc[T]));
    asm volatile("s_nop 4");
#pragma unroll
    for (int k = 0; k < DEPTH; ++k) B[k] = *(LAS const h8*)(ub_lo + 64 * (8 - (NU_HI - k)));
    __builtin_amdgcn_sched_barrier(0);
#pragma unroll
    for (int k = 0; k < STEPS; ++k) {
        const int nu = NU_HI - k;
#pragma unroll
        for (int T = 0; T < 8; ++T) {
            const int n = nu + 8 * T;
            if (n >= N0 && n < N1) asm volatile("v_mfma_f32_16x16x32_f16 %0, %1, %2, %0" : "+v"(acc[T]) : "v"(A[n - N0]), "v"(B[k % DEPTH]));
        }
        if (k + DEPTH < STEPS) B[k % DEPTH] = *(LAS const h8*)(ub_lo + 64 * (8 - (nu - DEPTH)));
        __builtin_amdgcn_sched_barrier(0);
    }
    asm volatile("s_nop 15\n\ts_nop 7");
#pragma unroll
    for (int T = T0; T < 8; ++T) asm volatile("" : "+v"(acc[T]));
}
DEV constexpr int pslot(int job, int T) { return job == 0 ? T : (job == 1 ? 7 + T : 11 + T); }

#define STAMP(i) do {} while (0)
#define PSTAMP(i) do {} while (0)
#define LSTAMP(i) STAMP(i)
#define STAMP_TID 0
}

__device__ const GatherTab gather_tab = make_gather_tab();

__global__ void __launch_bounds__(NT, 3)
s4d_moe_fused(const u16* __restrict__ x, const u16* __restrict__ pLogA, const u16* __restrict__ pAim,
              const u16* __restrict__ pCr, const u16* __restrict__ pCi, const u16* __restrict__ pLogDt,
              const u16* __restrict__ pDs, const u16* __restrict__ pWr, const u16* __restrict__ pWg,
              const u16* __restrict__ pWu, const u16* __restrict__ pWd, u16* __restrict__ out,
              unsigned char* __restrict__ ws) {
    __shared__ __attribute__((aligned(16))) unsigned char smem_[160 * 1024];
    LAS unsigned char* sm = (LAS unsigned char*)smem_;
    asm volatile("" :: "s"(x), "s"(pLogA), "s"(pAim), "s"(pCr), "s"(pCi), "s"(pLogDt), "s"(pDs), "s"(pWr), "s"(pWg), "s"(pWu), "s"(pWd),
                 "s"(out), "s"(ws));
    const int tid = threadIdx.x, lane = tid & 63;
    const int w = __builtin_amdgcn_readfirstlane(tid >> 6);
    const int b = blockIdx.x, grp = b & 7, slot = b >> 3;
    const bool is_conv = slot < 27;
    const int d0 = 80 * grp + 3 * slot;
    const int nch = is_conv ? (slot == 26 ? 2 : 3) : 0;
    const int f = (slot - 27) * 8 + grp;
    const __amdgpu_buffer_rsrc_t rws = __builtin_amdgcn_make_buffer_rsrc((void*)ws, 0, WS_END, 0x00020000);

    STAMP(0);
    if (tid == 0) { *(LAS u32*)(sm + M_CLR) = 0; *(LAS u32*)(sm + M_CCNT) = 0; }

    u32 xrow[5], wr[5];
    if (w < 8) {
        const u32* xr = (const u32*)(x + (size_t)(8 * b + w) * D);
#pragma unroll
        for (int i = 0; i < 5; ++i) xrow[i] = xr[lane + 64 * i];
    }
    u32 epoch = __builtin_amdgcn_raw_buffer_load_b32(rws, WS_EPOCH, 0, 0);
    auto get_tag = [&]() { u32 t = epoch; asm volatile("" : "+v"(t)); return t + 1u; };

    auto phase1 = [&]() {
        if (w < 8) {
            float m, s, rs;
            __builtin_amdgcn_s_setprio(2);
            ln_stats(xrow, m, s, rs);
#pragma unroll
            for (int i = 0; i < 5; ++i) {
                const u32 ub = ln_apply2(bf_lo(xrow[i]), bf_hi(xrow[i]), m, s, rs);
                *(LAS u32*)(sm + LD_UT + w * HT_ROW + (lane + 64 * i) * 4) = pk_h(bf_lo(ub), bf_hi(ub));
            }
            __builtin_amdgcn_s_setprio(0);
        }
        __syncthreads();
        STAMP(1);
        if (w >= 9) {
            __builtin_amdgcn_s_setprio(3);
            auto tr = [&](int grp64) {
                const int cp = 64 * grp64 + (lane >> 1) + 32 * (lane & 1);
                u32 v[8];
#pragma unroll
                for (int k = 0; k < 8; ++k) v[k] = *(LAS const u32*)(sm + LD_UT + k * HT_ROW + cp * 4);
                const bool odd = lane & 1;
                u32 keep[4], recv[4];
#pragma unroll
                for (int k = 0; k < 4; ++k) {
                    const u32 lo = __builtin_amdgcn_perm(v[2 * k + 1], v[2 * k], 0x05040100u), hi = __builtin_amdgcn_perm(v[2 * k + 1], v[2 * k], 0x07060302u);
                    keep[k] = odd ? hi : lo;
                    recv[k] = dppu<0xB1>(odd ? lo : hi);
                }
                const v4u a = {odd ? recv[0] : keep[0], odd ? recv[1] : keep[1], odd ? recv[2] : keep[2], odd ? recv[3] : keep[3]};
                const v4u c = {odd ? keep[0] : recv[0], odd ? keep[1] : recv[1], odd ? keep[2] : recv[2], odd ? keep[3] : recv[3]};
                st_sc1(rws, WS_UX + b * (D * 16) + (128 * grp64 + lane) * 16, a);
                st_sc1(rws, WS_UX + b * (D * 16) + (128 * grp64 + 64 + lane) * 16, c);
            };
            tr(w - 9);
            if (w < 11) tr(w - 6);
            PSTAMP(18);
            drain();
            PSTAMP(19);
            u32 old = 0;
            if (lane == 0) old = __hip_atomic_fetch_add((LAS u32*)(sm + M_CLR), 1u, __ATOMIC_RELAXED, __HIP_MEMORY_SCOPE_WORKGROUP);
            old = __builtin_amdgcn_readfirstlane(old);
            if (old == 2u && lane < 8) __builtin_amdgcn_raw_buffer_store_b32(get_tag(), rws, WS_G1 + lane * 1024 + b * 4, 0, 16);
            __builtin_amdgcn_s_setprio(0);
        }
    };
    auto clear_slots = [&]() {
        const v4u sv = {SENT, SENT, SENT, SENT};
        const int t = tid - 576;
#pragma unroll
        for (int i = 0; i < 3; ++i) st_sc1(rws, WS_HX + b * (D * 16) + (t + 192 * i) * 16, sv);
        if (w == 9) st_sc1(rws, WS_HX + b * (D * 16) + (576 + lane) * 16, sv);
        if (w == 10) {
            if (lane < 4) st_sc1(rws, WS_G3 + b * 64 + lane * 16, sv);
            if (!is_conv && f < 8 && lane >= 4 && lane < 6) st_sc1(rws, WS_IDX + f * 256 + (lane - 4) * 16, sv);
        }
        if (w == 11 && !is_conv && f < 24) st_sc1(rws, WS_ACT + (lane >> 3) * (F * 2) + f * 128 + (lane & 7) * 16, sv);
    };
    auto clear_rows = [&]() {
        const v4u sv = {SENT, SENT, SENT, SENT};
        const int t = tid - 576;
#pragma unroll
        for (int i = 0; i < 3; ++i) st_sc1(rws, WS_HROW + b * (D * 16) + (t + 192 * i) * 16, sv);
        if (w == 9) st_sc1(rws, WS_HROW + b * (D * 16) + (576 + lane) * 16, sv);
        drain();
    };
    if (w >= 9) clear_slots();

    v4u wf[20];
    if (is_conv) {
        u16 pla = 0, pai = 0, pcr[3] = {0, 0, 0}, pci[3] = {0, 0, 0}, pdt = 0;
        if (w < 9) {
            const int d = min(d0 + w / 3, D - 1), n = (w % 3) * 64 + lane;
            pla = pLogA[d * NS + n];
            pai = pAim[d * NS + n];
            pdt = pLogDt[d];
#pragma unroll
            for (int i = 0; i < 3; ++i) {
                pcr[i] = pCr[d * NS + i * 64 + lane];
                pci[i] = pCi[d * NS + i * 64 + lane];
            }
        }
        const int eo = tid / 3, ec = tid - 3 * eo;
        const u16 pdsk = pDs[min(d0 + ec, D - 1)];
        PSTAMP(16);
        u32 gi[6], gi2[6];
        {
            const v4u* gt = (const v4u*)gather_tab.v[1 + (tid & 255)];
            const v4u g0 = gt[0], g1 = gt[1];
            gi[0] = g0.x; gi[1] = g0.y; gi[2] = g0.z; gi[3] = g0.w; gi[4] = g1.x; gi[5] = g1.y;
            const v4u* gu = (const v4u*)gather_tab.v[257 + (lane & 1)];
            const v4u g2 = gu[0], g3 = gu[1];
            gi2[0] = g2.x; gi2[1] = g2.y; gi2[2] = g2.z; gi2[3] = g2.w; gi2[4] = g3.x; gi2[5] = g3.y;
        }
        PSTAMP(17);
        phase1();
        if (w < 9) {
            const int c = w / 3, n = (w % 3) * 64 + lane;
            const float dt = rbf(__expf(bf2f(pdt)));
            const float zr = -rbf(__expf(bf2f(pla))) * dt;
            const float zi = bf2f(pai) * dt;
            float sabs = 0.f;
#pragma unroll
            for (int i = 0; i < 3; ++i) sabs += __builtin_fabsf(bf2f(pcr[i]) * dt) + __builtin_fabsf(bf2f(pci[i]) * dt);
            int e = 14 - __builtin_amdgcn_frexp_expf(wave_sum(sabs));
            e = e > 100 ? 100 : (e < -100 ? -100 : e);
            if ((w % 3) == 0 && lane == 0) *(LAS int*)(sm + M_SCALE + 4 * c) = e;
            const float sc = __builtin_ldexpf(dt, e + 1);
            const u16 mcr = (w % 3) == 0 ? pcr[0] : ((w % 3) == 1 ? pcr[1] : pcr[2]);
            const u16 mci = (w % 3) == 0 ? pci[0] : ((w % 3) == 1 ? pci[1] : pci[2]);
            LAS u32* T = (LAS u32*)(sm + LD_TBL + c * TCH + n * 4);
            constexpr int RS = TROW / 4;
            const cf B1 = cexp_pos(zr, zi, 1.f), H1 = cexp_pos(zr, zi, 128.f), w8 = cexp_pos(zr, zi, 8.f);
            cf be = f2{bf2f(mcr) * sc, bf2f(mci) * sc};
#pragma unroll
            for (int j = 0; j < 16; ++j) {
                T[j * RS] = pk_h(be.x, -be.y);
                if (j < 15) be = cmul(be, w8);
            }
            const cf B2 = csq(B1), B3 = cmul(B2, B1), B4 = csq(B2), B5 = cmul(B4, B1), B6 = csq(B3), B7 = cmul(B4, B3);
            u32 hb[8];
            hb[0] = 0x00003C00u;
            hb[1] = c2h(B1); hb[2] = c2h(B2); hb[3] = c2h(B3); hb[4] = c2h(B4); hb[5] = c2h(B5); hb[6] = c2h(B6); hb[7] = c2h(B7);
            const cf H2 = csq(H1), H3 = cmul(H2, H1), H4 = csq(H2), H5 = cmul(H4, H1), H6 = csq(H3), H7 = cmul(H4, H3), H8 = csq(H4);
            const u32 h1 = c2h(H1), h2_ = c2h(H2), h3 = c2h(H3), h4 = c2h(H4), h5 = c2h(H5), h6 = c2h(H6), h7 = c2h(H7);
#pragma unroll
            for (int k = 0; k < 8; ++k) T[(16 + k) * RS] = hb[k];
            T[24 * RS] = h1;
#pragma unroll
            for (int k = 1; k < 8; ++k) T[(24 + k) * RS] = hcmul(h1, hb[k]);
            T[32 * RS] = h2_; T[33 * RS] = hcmul(h2_, hb[2]); T[34 * RS] = hcmul(h2_, hb[4]); T[35 * RS] = hcmul(h2_, hb[6]);
            T[36 * RS] = h3;  T[37 * RS] = hcmul(h3, hb[2]);  T[38 * RS] = hcmul(h3, hb[4]);  T[39 * RS] = hcmul(h3, hb[6]);
            T[40 * RS] = h4;  T[41 * RS] = hcmul(h4, hb[4]);
            T[42 * RS] = h5;  T[43 * RS] = hcmul(h5, hb[4]);
            T[44 * RS] = h6;  T[45 * RS] = hcmul(h6, hb[4]);
            T[46 * RS] = h7;  T[47 * RS] = hcmul(h7, hb[4]);
            T[48 * RS] = c2h(H8);
            T[49 * RS] = c2h(cmul(H8, H1));
            T[50 * RS] = c2h(csq(H5));
            T[51 * RS] = c2h(cmul(H8, H3));
            T[52 * RS] = c2h(csq(H6));
            T[53 * RS] = c2h(cmul(H8, H5));
            T[54 * RS] = c2h(csq(H7));
            T[55 * RS] = c2h(cmul(H8, H7));
            T[56 * RS] = c2h(csq(H8));
            if ((w % 3) == 0 && lane < 16) *(LAS u16*)(sm + LD_KD + c * (KD_LEN * 2) + (656 + lane) * 2) = 0;
        }
        if (w == 11) {
            const u32 tag = get_tag();
            __builtin_amdgcn_s_sleep(E1_DELAY);
            u32 sp = 0;
            while (true) {
                cbar();
                const v4u fl = ld_sc1(rws, WS_G1 + (b & 7) * 1024 + lane * 16);
                if (__all((fl.x == tag) & (fl.y == tag) & (fl.z == tag) & (fl.w == tag)) || ++sp > SPIN_CAP) break;
                __builtin_amdgcn_s_sleep(2);
            }
        }
        STAMP(2);
        __syncthreads();
        cbar();
        const v4u uv = ld_sc1(rws, WS_UX + eo * (D * 16) + min(d0 + ec, D - 1) * 16);
        STAMP(3);
        if (w < 9) {
            const int c = w / 3, tile = w % 3;
            const int j = lane & 15, g = lane >> 4;
            int row = 16 + 16 * tile + j;
            if (row > 56) row = 56;
            LAS const unsigned char* pa = sm + LD_TBL + c * TCH + row * TROW + g * 16;
            LAS const unsigned char* pb = sm + LD_TBL + c * TCH + j * TROW + g * 16;
            f4 acc0 = {0.f, 0.f, 0.f, 0.f}, acc1 = {0.f, 0.f, 0.f, 0.f};
#pragma unroll
            for (int t = 0; t < 6; ++t) {
                acc0 = __builtin_amdgcn_mfma_f32_16x16x32_f16(*(LAS const h8*)(pa + 128 * t), *(LAS const h8*)(pb + 128 * t), acc0, 0, 0, 0);
                acc1 = __builtin_amdgcn_mfma_f32_16x16x32_f16(*(LAS const h8*)(pa + 128 * t + 64), *(LAS const h8*)(pb + 128 * t + 64), acc1, 0, 0, 0);
            }
            const f4 acc = acc0 + acc1;
            const u32 p01 = pk_h(acc[0], acc[1]), p23 = pk_h(acc[2], acc[3]);
            LAS unsigned char* kd = sm + LD_KD + c * (KD_LEN * 2);
            if (tile == 0) {
                *(LAS v2u*)(kd + 2 * (128 * (g >> 1) + 4 * (g & 1) + 8 * j)) = v2u{p01, p23};
            } else if (tile == 1) {
                if (g < 2) {
                    *(LAS v2u*)(kd + 2 * (256 + 64 * g + 4 * j)) = v2u{p01, p23};
                } else {
                    *(LAS u32*)(kd + 2 * (384 + 64 * (g - 2) + 2 * j)) = p01;
                    *(LAS u32*)(kd + 2 * (384 + 64 * (g - 2) + 32 + 2 * j)) = p23;
                }
            } else {
                const int r0 = 4 * g;
                if (r0 <= 8) *(LAS u16*)(kd + 2 * (512 + 16 * r0 + j)) = (u16)p01;
                if (r0 + 3 <= 8) {
                    *(LAS u16*)(kd + 2 * (512 + 16 * (r0 + 1) + j)) = (u16)(p01 >> 16);
                    *(LAS u16*)(kd + 2 * (512 + 16 * (r0 + 2) + j)) = (u16)p23;
                    *(LAS u16*)(kd + 2 * (512 + 16 * (r0 + 3) + j)) = (u16)(p23 >> 16);
                }
            }
        }
        __syncthreads();
        STAMP(4);
        {
            auto gather = [&](int c, const u32 (&ix)[6], u32 (&P)[6]) {
                LAS const unsigned char* kd = sm + LD_KD + c * (KD_LEN * 2);
#pragma unroll
                for (int k = 0; k < 6; ++k)
                    P[k] = (u32) * (LAS const u16*)(kd + (ix[k] & 0xffffu)) | ((u32) * (LAS const u16*)(kd + (ix[k] >> 16)) << 16);
            };
            auto put = [&](int c, int a, const u32 (&P)[6]) {
                LAS unsigned char* dst = sm + LD_KC + c * (4 * KCS) + a * 16;
                *(LAS v4u*)(dst) = v4u{P[0], P[1], P[2], P[3]};
                *(LAS v4u*)(dst + KCS) = v4u{__builtin_amdgcn_alignbit(P[1], P[0], 16), __builtin_amdgcn_alignbit(P[2], P[1], 16),
                                             __builtin_amdgcn_alignbit(P[3], P[2], 16), __builtin_amdgcn_alignbit(P[4], P[3], 16)};
                *(LAS v4u*)(dst + 2 * KCS) = v4u{P[1], P[2], P[3], P[4]};
                *(LAS v4u*)(dst + 3 * KCS) = v4u{__builtin_amdgcn_alignbit(P[2], P[1], 16), __builtin_amdgcn_alignbit(P[3], P[2], 16),
                                                 __builtin_amdgcn_alignbit(P[4], P[3], 16), __builtin_amdgcn_alignbit(P[5], P[4], 16)};
            };
            if (w < 8) {
#pragma unroll
                for (int i = 0; i < 5; ++i) *(LAS u32*)(sm + LD_XR + w * 1280 + (lane + 64 * i) * 4) = xrow[i];
            }
            u32 P[6], Q[6];
            gather(tid >> 8, gi, P);
            if (w == 11) gather((lane >> 1) % 3, gi2, Q);
            put(tid >> 8, 1 + (tid & 255), P);
            if (w == 11) {
                if (lane < 6) put(lane >> 1, 257 + (lane & 1), Q);
            } else if (w == 10) {
                for (int i = lane; i < 72; i += 64) {
                    const int cs = i / 6, k = i % 6;
                    *(LAS v4u*)(sm + LD_KC + (cs >> 2) * (4 * KCS) + (cs & 3) * KCS + (k == 0 ? 0 : 258 + k) * 16) = v4u{0, 0, 0, 0};
                }
            } else if (w == 9) {
                for (int i = lane; i < 102; i += 64) {
                    const int c = i / 34, k = i % 34;
                    const int el = k < 32 ? 8 * k : 2304 + 8 * (k - 32);
                    *(LAS v4u*)(sm + LD_UU + c * (ULEN * 2) + el * 2) = v4u{0, 0, 0, 0};
                }
            }
        }
        __syncthreads();
        STAMP(5);
        int job = -1, jc = 0;
        if ((w & 3) == 0) { job = 2; jc = w >> 2; }
        else if (w < 8) { job = (w >> 2); jc = (w & 3) - 1; }
        h8 A[29];
        const u32 kc = LD_KC + jc * (4 * KCS);
        if (job == 0) conv_load_frags<0, 16>(kc, sm, lane, A);
        else if (job == 1) conv_load_frags<16, 36>(kc, sm, lane, A);
        else if (job == 2) conv_load_frags<36, 65>(kc, sm, lane, A);
        STAMP(6);
        *(LAS v4u*)(sm + LD_UU + ec * (ULEN * 2) + (256 + 8 * eo) * 2) = uv;
        STAMP(7);
        __syncthreads();
        STAMP(8);
        if (job >= 0) {
            f4 acc[8];
#pragma unroll
            for (int T = 0; T < 8; ++T) acc[T] = f4{0.f, 0.f, 0.f, 0.f};
            const int p = lane & 15, g = lane >> 4;
            u32 uba = LD_UU + jc * (ULEN * 2) + (16 * p + 8 * g) * 2;
            asm volatile("" : "+v"(uba));
            LAS const unsigned char* ub = sm + uba;
            LAS unsigned char* part = sm + LD_PART + jc * PT_CH + g * PT_G + p * 16;
            if (job == 0) {
                conv_run<0, 16, DEP_L>(A, ub, acc);
#pragma unroll
                for (int T = 0; T < 8; ++T) *(LAS f4*)(part + pslot(0, T) * PT) = acc[T];
            } else if (job == 1) {
                conv_run<16, 36, DEP_M>(A, ub, acc);
#pragma unroll
                for (int T = 1; T < 8; ++T) *(LAS f4*)(part + pslot(1, T) * PT) = acc[T];
            } else {
                conv_run<36, 65, DEP_H>(A, ub, acc);
#pragma unroll
                for (int T = 4; T < 8; ++T) *(LAS f4*)(part + pslot(2, T) * PT) = acc[T];
            }
        } else {
            clear_rows();
        }
        STAMP(9);
        __syncthreads();
        STAMP(10);
        {
            const int T = eo >> 5, p = (eo >> 1) & 15, g0 = (eo & 1) * 2;
            LAS const unsigned char* part = sm + LD_PART + ec * PT_CH + g0 * PT_G + p * 16;
            const int sM = T >= 1 ? pslot(1, T) : 0, sH = T >= 4 ? pslot(2, T) : 0;
            const float kM = T >= 1 ? 1.f : 0.f, kH = T >= 4 ? 1.f : 0.f;
            const f4 l0 = *(LAS const f4*)(part + T * PT), l1 = *(LAS const f4*)(part + T * PT + PT_G);
            const f4 m0 = *(LAS const f4*)(part + sM * PT), m1 = *(LAS const f4*)(part + sM * PT + PT_G);
            const f4 h0 = *(LAS const f4*)(part + sH * PT), h1 = *(LAS const f4*)(part + sH * PT + PT_G);
            const int e = *(LAS const int*)(sm + M_SCALE + 4 * (ec < 2 ? ec : 2));
            const float isc = __builtin_ldexpf(1.f, -e);
            const float dsk = bf2f(pdsk);
            const f4 y0 = (l0 + m0 * kM + h0 * kH) * isc, y1 = (l1 + m1 * kM + h1 * kH) * isc;
            const float y[8] = {y0[0], y0[1], y0[2], y0[3], y1[0], y1[1], y1[2], y1[3]};
            const v4u ur = *(LAS const v4u*)(sm + LD_UU + ec * (ULEN * 2) + (256 + 8 * eo) * 2);
            const u32 uu[4] = {ur.x, ur.y, ur.z, ur.w};
            u32 o[4];
#pragma unroll
            for (int k = 0; k < 4; ++k) {
                const h2 u2 = as_h2(uu[k]);
                const u32 yb = pk_bf(y[2 * k], y[2 * k + 1]), ud = pk_bf((float)u2.x * dsk, (float)u2.y * dsk);
                o[k] = pk_bf(bf_lo(yb) + bf_lo(ud), bf_hi(yb) + bf_hi(ud));
            }
            if (ec < nch) st_sc1(rws, WS_HX + eo * (D * 16) + (d0 + ec) * 16, v4u{o[0], o[1], o[2], o[3]});
            STAMP(11);
            drain();
        }
    } else {
        v4u rw[16];
        auto load_weights = [&]() {
            if (f < 24) {
#pragma unroll
                for (int i = 0; i < 7; ++i) {
                    const int p = min(tid + NT * i, 5119);
                    rw[i] = *(const v4u*)(pWg + (size_t)(p >> 3) * F + 64 * f + 8 * (p & 7));
                    rw[8 + i] = *(const v4u*)(pWu + (size_t)(p >> 3) * F + 64 * f + 8 * (p & 7));
                }
            } else if (f < 34) {
#pragma unroll
                for (int i = 0; i < 16; ++i) {
                    const int p = tid + NT * i;
                    rw[i] = *(const v4u*)(pWd + (size_t)(p >> 3) * D + 64 * (f - 24) + 8 * (p & 7));
                }
            }
        };
        phase1();
        load_weights();
        const int g = lane >> 4, q = (lane & 15) >> 2, pp = lane & 3;
        if (f < 24) {
#pragma unroll
            for (int rnd = 0; rnd < 2; ++rnd) {
#pragma unroll
                for (int i = 0; i < 7; ++i) {
                    const int p = tid + NT * i;
                    if (p < 5120) *(LAS v4u*)(sm + LF_W + p * 16) = rw[8 * rnd + i];
                }
                __syncthreads();
                if (w < 8 && (w >> 2) == rnd) {
                    LAS const unsigned char* base = sm + LF_W + (w & 3) * 32 + pp * 8;
#pragma unroll
                    for (int s = 0; s < 20; ++s) {
                        const int k0 = 32 * s + 8 * g + q;
                        const s4 a = __builtin_amdgcn_ds_read_tr16_b64_v4i16((LAS s4*)(base + k0 * 128));
                        const s4 c = __builtin_amdgcn_ds_read_tr16_b64_v4i16((LAS s4*)(base + (k0 + 4) * 128));
                        const v2u a2 = __builtin_bit_cast(v2u, a), c2 = __builtin_bit_cast(v2u, c);
                        wf[s] = v4u{a2.x, a2.y, c2.x, c2.y};
                    }
                }
                __syncthreads();
            }
        } else if (f < 34) {
#pragma unroll
            for (int rnd = 0; rnd < 2; ++rnd) {
#pragma unroll
                for (int i = 0; i < 8; ++i) *(LAS v4u*)(sm + LF_W + (tid + NT * i) * 16) = rw[8 * rnd + i];
                __syncthreads();
                LAS const unsigned char* base = sm + LF_W + (w & 3) * 32 + pp * 8;
#pragma unroll
                for (int s = 0; s < 16; ++s) {
                    const int ks = 16 * (w >> 2) + s;
                    if ((ks >= 24) == (rnd == 1)) {
                        const int k0 = 32 * (ks - 24 * rnd) + 8 * g + q;
                        const s4 a = __builtin_amdgcn_ds_read_tr16_b64_v4i16((LAS s4*)(base + k0 * 128));
                        const s4 c = __builtin_amdgcn_ds_read_tr16_b64_v4i16((LAS s4*)(base + (k0 + 4) * 128));
                        const v2u a2 = __builtin_bit_cast(v2u, a), c2 = __builtin_bit_cast(v2u, c);
                        wf[s] = v4u{a2.x, a2.y, c2.x, c2.y};
                    }
                }
                __syncthreads();
            }
        }
        if (w >= 9) clear_rows();
        if (w < 8) {
#pragma unroll
            for (int i = 0; i < 5; ++i) *(LAS u32*)(sm + LD_XR + w * 1280 + (lane + 64 * i) * 4) = xrow[i];
        }
        __syncthreads();
        if (w == 0) {
            u32 sp = 0;
            while (true) {
                cbar();
                const u32 v = __builtin_amdgcn_raw_buffer_load_b32(rws, WS_HX + b * (D * 16), 0, 16);
                if (v != SENT || ++sp > SPIN_CAP) break;
                __builtin_amdgcn_s_sleep(4);
            }
        }
    }
    if (w < 8) {
#pragma unroll
        for (int i = 0; i < 5; ++i) wr[i] = ((const u32*)pWr)[lane + 64 * i];
    }
    __syncthreads();
    STAMP(12);

    if (tid < D) {
        v4u v;
        u32 sp = 0;
        while (true) {
            cbar();
            v = ld_sc1(rws, WS_HX + b * (D * 16) + tid * 16);
            const bool ok = (v.x != SENT) & (v.y != SENT) & (v.z != SENT) & (v.w != SENT);
            if (__all(ok) || ++sp > SPIN_CAP) break;
        }
        STAMP(13);
        const bool odd = tid & 1;
        const u32 s0 = odd ? v.x : v.z, s1 = odd ? v.y : v.w;
        const u32 r0 = dppu<0xB1>(s0), r1 = dppu<0xB1>(s1);
        const u32 k0 = odd ? v.z : v.x, k1 = odd ? v.w : v.y;
        const u32 lo0 = odd ? r0 : k0, hi0 = odd ? k0 : r0, lo1 = odd ? r1 : k1, hi1 = odd ? k1 : r1;
        LAS u32* dst = (LAS u32*)(sm + LD_HT + (odd ? 4 * HT_ROW : 0) + (tid >> 1) * 4);
        dst[0 * (HT_ROW / 4)] = __builtin_amdgcn_perm(hi0, lo0, 0x05040100u);
        dst[1 * (HT_ROW / 4)] = __builtin_amdgcn_perm(hi0, lo0, 0x07060302u);
        dst[2 * (HT_ROW / 4)] = __builtin_amdgcn_perm(hi1, lo1, 0x05040100u);
        dst[3 * (HT_ROW / 4)] = __builtin_amdgcn_perm(hi1, lo1, 0x07060302u);
    }
    __syncthreads();
    u32 orow[5];
    const int mytok = 8 * b + w;
    if (w < 8) {
        u32 hv[5];
#pragma unroll
        for (int i = 0; i < 5; ++i) {
            const u32 a = *(LAS const u32*)(sm + LD_HT + w * HT_ROW + (lane + 64 * i) * 4);
            const u32 xv = *(LAS const u32*)(sm + LD_XR + w * 1280 + (lane + 64 * i) * 4);
            hv[i] = pk_bf(bf_lo(xv) + bf_lo(a), bf_hi(xv) + bf_hi(a));
        }
        float m, s, rs;
        ln_stats(hv, m, s, rs);
        float racc = 0.f;
#pragma unroll
        for (int i = 0; i < 5; ++i) {
            const u32 z = ln_apply2(bf_lo(hv[i]), bf_hi(hv[i]), m, s, rs);
            racc = __builtin_fmaf(bf_lo(z), bf_lo(wr[i]), __builtin_fmaf(bf_hi(z), bf_hi(wr[i]), racc));
            orow[i] = pk_bf(bf_lo(hv[i]) + bf_lo(z), bf_hi(hv[i]) + bf_hi(z));
        }
        const float r = wave_sum(racc);
        if (lane == 0) {
            v2u g;
            g.x = (__float_as_uint(m) >> 16) | (__float_as_uint(s) & 0xffff0000u);
            g.y = pk_bf(r, 0.f) & 0xffffu;
            __builtin_amdgcn_raw_buffer_store_b64(g, rws, WS_G3 + mytok * 8, 0, 16);
        }
        STAMP(14);
#pragma unroll
        for (int i = 0; i < 5; ++i)
            __builtin_amdgcn_raw_buffer_store_b32(hv[i], rws, WS_HROW + mytok * (D * 2) + (lane + 64 * i) * 4, 0, 16);
    }
    STAMP(15);

    if (!is_conv && f < 34) {
        u32 key[4], msv[4];
        if (w < 8) {
            __builtin_amdgcn_s_sleep(8);
            v4u g0, g1;
            u32 sp = 0;
            while (true) {
                cbar();
                g0 = ld_sc1(rws, WS_G3 + tid * 32);
                g1 = ld_sc1(rws, WS_G3 + tid * 32 + 16);
                const bool ok = (g0.y != SENT) & (g0.w != SENT) & (g1.y != SENT) & (g1.w != SENT);
                if (__all(ok) || ++sp > SPIN_CAP) break;
            }
            LSTAMP(16);
            const u32 rb[4] = {g0.y, g0.w, g1.y, g1.w};
            msv[0] = g0.x; msv[1] = g0.z; msv[2] = g1.x; msv[3] = g1.z;
#pragma unroll
            for (int j = 0; j < 4; ++j) {
                const u32 o = (rb[j] & 0x8000u) ? (~rb[j] & 0xffffu) : (rb[j] | 0x8000u);
                key[j] = (o << 11) | (u32)(2047 - (4 * tid + j));
            }
            const u32 mx = wave_max(umax(umax(key[0], key[1]), umax(key[2], key[3])));
            if (lane == 0) *(LAS u32*)(sm + M_WMAX + 4 * w) = mx;
        }
        __syncthreads();
        if (w < 8) {
            const v4u a = *(LAS const v4u*)(sm + M_WMAX), c = *(LAS const v4u*)(sm + M_WMAX + 16);
            const u32 thr = min(min(min(a.x, a.y), min(a.z, a.w)), min(min(c.x, c.y), min(c.z, c.w)));
            u64 bal[4];
            int tot = 0;
#pragma unroll
            for (int j = 0; j < 4; ++j) { bal[j] = __ballot(key[j] >= thr); tot += __builtin_popcountll(bal[j]); }
            u32 base = 0;
            if (lane == 0) base = __hip_atomic_fetch_add((LAS u32*)(sm + M_CCNT), (u32)tot, __ATOMIC_RELAXED, __HIP_MEMORY_SCOPE_WORKGROUP);
            base = __builtin_amdgcn_readfirstlane(base);
#pragma unroll
            for (int j = 0; j < 4; ++j) {
                const u32 pos = base + __builtin_amdgcn_mbcnt_hi((u32)(bal[j] >> 32), __builtin_amdgcn_mbcnt_lo((u32)bal[j], 0));
                if (key[j] >= thr) *(LAS v2u*)(sm + LF_CAND + pos * 8) = v2u{key[j], msv[j]};
                base += __builtin_popcountll(bal[j]);
            }
        }
        __syncthreads();
        {
            const u32 cnt = *(LAS const u32*)(sm + M_CCNT);
            for (u32 i0 = 64 * w; i0 < cnt; i0 += NT) {
                const u32 i = i0 + lane;
                const v2u me = *(LAS const v2u*)(sm + LF_CAND + min(i, cnt - 1) * 8);
                u32 rank = 0;
                for (u32 j0 = 0; j0 < cnt; j0 += 64) {
                    const u32 ot = (j0 + lane) < cnt ? *(LAS const u32*)(sm + LF_CAND + (j0 + lane) * 8) : 0u;
                    const u32 nj = min(64u, cnt - j0);
                    for (u32 j = 0; j < nj; j += 4) {
                        rank += (u32)__builtin_amdgcn_readlane(ot, j) > me.x;
                        rank += (u32)__builtin_amdgcn_readlane(ot, j + 1) > me.x;
                        rank += (u32)__builtin_amdgcn_readlane(ot, j + 2) > me.x;
                        rank += (u32)__builtin_amdgcn_readlane(ot, j + 3) > me.x;
                    }
                }
                if (i < cnt && rank < 8) {
                    const u32 rbits = me.x >> 11;
                    const u32 rb = (rbits & 0x8000u) ? (rbits & 0x7fffu) : (~rbits & 0xffffu);
                    *(LAS v4u*)(sm + M_SEL + rank * 16) = v4u{2047u - (me.x & 2047u), me.y, rb, 0u};
                }
            }
        }
        __syncthreads();
        LSTAMP(17);
        if (tid < D) {
            const int k = tid / 80, ch = tid % 80;
            const v4u sel = *(LAS const v4u*)(sm + M_SEL + k * 16);
            v4u hv;
            u32 sp = 0;
            while (true) {
                cbar();
                hv = ld_sc1(rws, WS_HROW + (int)(sel.x & (L - 1)) * (D * 2) + ch * 16);
                const bool ok = (hv.x != SENT) & (hv.y != SENT) & (hv.z != SENT) & (hv.w != SENT);
                if (__all(ok) || ++sp > SPIN_CAP) break;
            }
            const float m = bf_lo(sel.y), s = bf_hi(sel.y), rs = __builtin_amdgcn_rcpf(s);
            const u32 hh[4] = {hv.x, hv.y, hv.z, hv.w};
            u32 zz[4];
#pragma unroll
            for (int i = 0; i < 4; ++i) zz[i] = ln_apply2(bf_lo(hh[i]), bf_hi(hh[i]), m, s, rs);
            *(LAS v4u*)(sm + LF_Z + k * HT_ROW + ch * 16) = v4u{zz[0], zz[1], zz[2], zz[3]};
            *(LAS v4u*)(sm + LF_H + k * HT_ROW + ch * 16) = hv;
        } else if (w == 11 && f < 8 && lane < 2) {
            const v4u s0 = *(LAS const v4u*)(sm + M_SEL + lane * 64), s1 = *(LAS const v4u*)(sm + M_SEL + lane * 64 + 16),
                      s2 = *(LAS const v4u*)(sm + M_SEL + lane * 64 + 32), s3 = *(LAS const v4u*)(sm + M_SEL + lane * 64 + 48);
            st_sc1(rws, WS_IDX + f * 256 + lane * 16, v4u{s0.x, s1.x, s2.x, s3.x});
        }
        __syncthreads();
        LSTAMP(18);
        const int row = lane & 7, g = lane >> 4;
        if (f < 24) {
            f4 acc = {0.f, 0.f, 0.f, 0.f};
            if (w < 8) {
                f4 acc1 = {0.f, 0.f, 0.f, 0.f};
                LAS const unsigned char* zb = sm + LF_Z + row * HT_ROW + 16 * g;
#pragma unroll
                for (int s = 0; s < 20; s += 2) {
                    acc = __builtin_amdgcn_mfma_f32_16x16x32_bf16(*(LAS const b8*)(zb + 64 * s), __builtin_bit_cast(b8, wf[s]), acc, 0, 0, 0);
                    acc1 = __builtin_amdgcn_mfma_f32_16x16x32_bf16(*(LAS const b8*)(zb + 64 * s + 64), __builtin_bit_cast(b8, wf[s + 1]), acc1, 0, 0, 0);
                }
                acc += acc1;
                if (w >= 4) *(LAS f4*)(sm + LF_RED + (w - 4) * 1024 + lane * 16) = acc;
            }
            __syncthreads();
            if (w < 4) {
                const f4 up = *(LAS const f4*)(sm + LF_RED + w * 1024 + lane * 16);
                float a[4];
#pragma unroll
                for (int r = 0; r < 4; ++r) {
                    const float gb = rbf(acc[r]);
                    const float sg = rbf(gb * __builtin_amdgcn_rcpf(1.f + __expf(-gb)));
                    a[r] = sg * rbf(up[r]);
                }
                const u32 p01 = pk_bf(a[0], a[1]), p23 = pk_bf(a[2], a[3]);
                const bool odd = lane & 1;
                const u32 rcv = dppu<0xB1>(odd ? p01 : p23), own = odd ? p23 : p01;
                const u32 lo = odd ? rcv : own, hi = odd ? own : rcv;
                if (g < 2) {
                    const int tok = 4 * g + (odd ? 2 : 0);
                    const int off = WS_ACT + tok * (F * 2) + (64 * f + 16 * w + (lane & 14)) * 2;
                    __builtin_amdgcn_raw_buffer_store_b32(__builtin_amdgcn_perm(hi, lo, 0x05040100u), rws, off, 0, 16);
                    __builtin_amdgcn_raw_buffer_store_b32(__builtin_amdgcn_perm(hi, lo, 0x07060302u), rws, off + F * 2, 0, 16);
                }
            }
            LSTAMP(19);
        } else {
            {
                v4u a0, a1;
                u32 sp = 0;
                __builtin_amdgcn_s_sleep(8);
                while (true) {
                    cbar();
                    a0 = ld_sc1(rws, WS_ACT + tid * 16);
                    a1 = ld_sc1(rws, WS_ACT + (tid + NT) * 16);
                    const bool ok = (a0.x != SENT) & (a0.y != SENT) & (a0.z != SENT) & (a0.w != SENT) & (a1.x != SENT) &
                                    (a1.y != SENT) & (a1.z != SENT) & (a1.w != SENT);
                    if (__all(ok) || ++sp > SPIN_CAP) break;
                }
                *(LAS v4u*)(sm + LF_ACT + (tid / 192) * ACT_ROW + (tid % 192) * 16) = a0;
                *(LAS v4u*)(sm + LF_ACT + ((tid + NT) / 192) * ACT_ROW + ((tid + NT) % 192) * 16) = a1;
            }
            __syncthreads();
            LSTAMP(20);
            f4 acc = {0.f, 0.f, 0.f, 0.f}, acc1 = {0.f, 0.f, 0.f, 0.f};
            LAS const unsigned char* ab = sm + LF_ACT + row * ACT_ROW + 1024 * (w >> 2) + 16 * g;
#pragma unroll
            for (int s = 0; s < 16; s += 2) {
                acc = __builtin_amdgcn_mfma_f32_16x16x32_bf16(*(LAS const b8*)(ab + 64 * s), __builtin_bit_cast(b8, wf[s]), acc, 0, 0, 0);
                acc1 = __builtin_amdgcn_mfma_f32_16x16x32_bf16(*(LAS const b8*)(ab + 64 * s + 64), __builtin_bit_cast(b8, wf[s + 1]), acc1, 0, 0, 0);
            }
            acc += acc1;
            *(LAS f4*)(sm + LF_RED + w * 1024 + lane * 16) = acc;
            __syncthreads();
            if (tid < 512) {
                const int tok = tid >> 6, col = tid & 63, tile = col >> 4;
                const int off = ((tok >> 2) * 16 + (col & 15)) * 16 + (tok & 3) * 4;
                const float y = *(LAS const float*)(sm + LF_RED + tile * 1024 + off) + *(LAS const float*)(sm + LF_RED + (4 + tile) * 1024 + off) +
                                *(LAS const float*)(sm + LF_RED + (8 + tile) * 1024 + off);
                const v4u sel = *(LAS const v4u*)(sm + M_SEL + tok * 16);
                const int d = 64 * (f - 24) + col;
                const float z = bf2f(*(LAS const u16*)(sm + LF_Z + tok * HT_ROW + d * 2));
                const float h = bf2f(*(LAS const u16*)(sm + LF_H + tok * HT_ROW + d * 2));
                const float r = bf2f((u16)sel.z);
                const float sg = rbf(__builtin_amdgcn_rcpf(1.f + __expf(-r)));
                const float upd = rbf(rbf(y) * sg);
                out[(size_t)(sel.x & (L - 1)) * D + d] = (u16)pk_bf(h + rbf(z + upd), 0.f);
            }
        }
    }

    STAMP(21);
    if (w < 8) {
        v4u i0, i1;
        u32 sp = 0;
        while (true) {
            cbar();
            i0 = ld_sc1(rws, WS_IDX + (b & 7) * 256);
            i1 = ld_sc1(rws, WS_IDX + (b & 7) * 256 + 16);
            const bool ok = (i0.x != SENT) & (i0.y != SENT) & (i0.z != SENT) & (i0.w != SENT) & (i1.x != SENT) & (i1.y != SENT) &
                            (i1.z != SENT) & (i1.w != SENT);
            if (__all(ok) || ++sp > SPIN_CAP) break;
            __builtin_amdgcn_s_sleep(16);
        }
        const u32 t = (u32)mytok;
        const bool routed = (i0.x == t) | (i0.y == t) | (i0.z == t) | (i0.w == t) | (i1.x == t) | (i1.y == t) | (i1.z == t) | (i1.w == t);
        if (!routed) {
            u32* o = (u32*)(out + (size_t)mytok * D);
#pragma unroll
            for (int i = 0; i < 5; ++i) o[lane + 64 * i] = orow[i];
        }
        if (b == 0 && tid == 0) *(u32*)(ws + WS_EPOCH) = get_tag();
    }
}

extern "C" void kernel_launch(void* const* d_in, const int* in_sizes, int n_in, void* d_out, int out_size, void* d_ws,
                              size_t ws_size, hipStream_t stream) {
    (void)in_sizes; (void)n_in; (void)out_size; (void)ws_size;
    s4d_moe_fused<<<256, NT, 0, stream>>>((const u16*)d_in[0], (const u16*)d_in[1], (const u16*)d_in[2], (const u16*)d_in[3],
                                          (const u16*)d_in[4], (const u16*)d_in[5], (const u16*)d_in[6],
                                          (const u16*)d_in[8], (const u16*)d_in[9], (const u16*)d_in[10],
                                          (const u16*)d_in[11], (u16*)d_out, (unsigned char*)d_ws);
}
```

```cpp
#include <hip/hip_runtime.h>
#include <stdint.h>

typedef unsigned int u32;
typedef unsigned long long u64;
typedef unsigned short u16;
typedef u32 v4u __attribute__((ext_vector_type(4)));
typedef u32 v2u __attribute__((ext_vector_type(2)));
typedef float f4 __attribute__((ext_vector_type(4)));
typedef float f2 __attribute__((ext_vector_type(2)));
typedef _Float16 h2 __attribute__((ext_vector_type(2)));
typedef _Float16 h8 __attribute__((ext_vector_type(8)));
typedef __bf16 b2 __attribute__((ext_vector_type(2)));
typedef __bf16 b8 __attribute__((ext_vector_type(8)));
typedef short s4 __attribute__((ext_vector_type(4)));
#define LAS __attribute__((address_space(3)))
#define DEV __device__ __forceinline__

namespace {

constexpr int L = 2048, D = 640, NS = 192, F = 1536;
constexpr int NT = 768;
constexpr u32 SENT = 0xFFFFFFFFu;
constexpr u32 SPIN_CAP = 1u << 16;
#define DEP_L 4
#define DEP_M 4
#define DEP_H 4
#define P3_DELAY 0
#define P3_BACK 0
#define G3_DELAY 8
#define G3_BACK 0
#define ACT_DELAY 8
#define ACT_BACK 0
#define OUT_AUX 0
#define FFN_IDLE 4
#define HX_AUX 16
#define HROW_DELAY 0
#define E1_DELAY 12

constexpr int WS_EPOCH = 0;
constexpr int WS_G1 = 4096;
constexpr int WS_G3 = WS_G1 + 8192;
constexpr int WS_IDX = WS_G3 + 16384;
constexpr int WS_ACT = WS_IDX + 2048;
constexpr int WS_UX = 65536;
constexpr int HXB = 216 * 64;
constexpr int WS_HX = WS_UX + L * D * 2;
constexpr int WS_HROW = WS_HX + 256 * HXB;
constexpr int WS_END = WS_HROW + L * D * 2;
constexpr int WS_DBG = 16 << 20;

constexpr int TCH = 800;
constexpr int TROW = 3 * TCH;
constexpr int LD_TBL = 0;
constexpr int KD_LEN = 672;
constexpr int LD_KD = 136800;
constexpr int LD_UT = LD_KD + 3 * KD_LEN * 2;
constexpr int LD_MISC = 153600;
constexpr int KCS = 4416;
constexpr int LD_KC = 0;
constexpr int ULEN = 2320;
constexpr int PT_G = 320;
constexpr int PT = 4 * PT_G;
constexpr int PT_CH = 19 * PT + 48;
constexpr int LD_PART = 0;
constexpr int LD_UU = 99328;
constexpr int LD_XR = 114688;
constexpr int LD_HT = 0;
constexpr int HT_ROW = 648 * 2;
constexpr int M_CLR = LD_MISC;
constexpr int M_CCNT = LD_MISC + 4;
constexpr int M_P1 = LD_MISC + 8;
constexpr int M_P3 = LD_MISC + 12;
constexpr int M_G3 = LD_MISC + 256;
constexpr int M_SCALE = LD_MISC + 16;
constexpr int M_WMAX = LD_MISC + 64;
constexpr int M_SEL = LD_MISC + 128;
constexpr int M_STAMP = LD_MISC + 512;
constexpr int Z0 = 2064;

constexpr int LF_W = 0;
constexpr int LF_CAND = 16384;
constexpr int LF_Z = 32768;
constexpr int LF_H = LF_Z + 8 * HT_ROW;
constexpr int LF_RED = 57344;
constexpr int LF_ACT = 73728;
constexpr int ACT_ROW = 3088;

DEV float bf_lo(u32 v) { return __uint_as_float(v << 16); }
DEV float bf_hi(u32 v) { return __uint_as_float(v & 0xffff0000u); }
DEV float bf2f(u16 v) { return __uint_as_float((u32)v << 16); }
DEV u32 pk_bf(float a, float b) {
    f2 v = {a, b};
    return __builtin_bit_cast(u32, __builtin_convertvector(v, b2));
}
DEV h2 as_h2(u32 v) { return __builtin_bit_cast(h2, v); }
DEV float rbf(float a) { return bf_lo(pk_bf(a, a)); }
DEV u32 pk_h(float a, float b) {
    f2 v = {a, b};
    return __builtin_bit_cast(u32, __builtin_convertvector(v, h2));
}
template <int CTRL> DEV float dppf(float v) {
    return __int_as_float(__builtin_amdgcn_update_dpp(0, __float_as_int(v), CTRL, 0xf, 0xf, true));
}
template <int CTRL> DEV u32 dppu(u32 v) { return (u32)__builtin_amdgcn_update_dpp(0, (int)v, CTRL, 0xf, 0xf, true); }
DEV float wave_sum(float v) {
    v += dppf<0xB1>(v);
    v += dppf<0x4E>(v);
    v += dppf<0x141>(v);
    v += dppf<0x140>(v);
    auto r = __builtin_amdgcn_permlane16_swap(__float_as_uint(v), __float_as_uint(v), false, false);
    v = __uint_as_float(r[0]) + __uint_as_float(r[1]);
    auto q = __builtin_amdgcn_permlane32_swap(__float_as_uint(v), __float_as_uint(v), false, false);
    return __uint_as_float(q[0]) + __uint_as_float(q[1]);
}
DEV u32 umax(u32 a, u32 b) { return a > b ? a : b; }
DEV u32 wave_max(u32 v) {
    v = umax(v, dppu<0xB1>(v));
    v = umax(v, dppu<0x4E>(v));
    v = umax(v, dppu<0x141>(v));
    v = umax(v, dppu<0x140>(v));
    auto r = __builtin_amdgcn_permlane16_swap(v, v, false, false);
    v = umax(r[0], r[1]);
    auto q = __builtin_amdgcn_permlane32_swap(v, v, false, false);
    return umax(q[0], q[1]);
}

typedef f2 cf;
DEV cf cmul(cf a, cf b) { return __builtin_elementwise_fma(f2{a.y, a.y}, f2{-b.y, b.x}, f2{a.x, a.x} * b); }
DEV cf csq(cf a) { return cmul(a, a); }
DEV cf cexp_pos(float zr, float zi, float pos) {
    const float mag = __builtin_amdgcn_exp2f(zr * pos * 1.4426950408889634f);
    const float rev = __builtin_amdgcn_fractf(zi * pos * 0.15915494309189535f);
    return f2{mag * __builtin_amdgcn_cosf(rev), mag * __builtin_amdgcn_sinf(rev)};
}
DEV u32 c2h(cf a) { return pk_h(a.x, a.y); }
DEV u32 hcmul(u32 a_, u32 b_) {
    const h2 a = __builtin_bit_cast(h2, a_), b = __builtin_bit_cast(h2, b_);
    const h2 t = h2{a.x, a.x} * b;
    const h2 r = __builtin_elementwise_fma(h2{a.y, a.y}, h2{-b.y, b.x}, t);
    return __builtin_bit_cast(u32, r);
}

DEV void wave_sum2(float& a, float& b) {
    a += dppf<0xB1>(a);  b += dppf<0xB1>(b);
    a += dppf<0x4E>(a);  b += dppf<0x4E>(b);
    a += dppf<0x141>(a); b += dppf<0x141>(b);
    a += dppf<0x140>(a); b += dppf<0x140>(b);
    auto r = __builtin_amdgcn_permlane16_swap(__float_as_uint(a), __float_as_uint(a), false, false);
    auto t = __builtin_amdgcn_permlane16_swap(__float_as_uint(b), __float_as_uint(b), false, false);
    a = __uint_as_float(r[0]) + __uint_as_float(r[1]);
    b = __uint_as_float(t[0]) + __uint_as_float(t[1]);
    auto q = __builtin_amdgcn_permlane32_swap(__float_as_uint(a), __float_as_uint(a), false, false);
    auto u = __builtin_amdgcn_permlane32_swap(__float_as_uint(b), __float_as_uint(b), false, false);
    a = __uint_as_float(q[0]) + __uint_as_float(q[1]);
    b = __uint_as_float(u[0]) + __uint_as_float(u[1]);
}
DEV void ln_stats(const u32 (&v)[5], float& m, float& s, float& rs) {
    const float c = bf_lo(__builtin_amdgcn_readfirstlane(v[0]));
    float s1 = 0.f, s2 = 0.f;
#pragma unroll
    for (int i = 0; i < 5; ++i) {
        const float a = bf_lo(v[i]) - c, b = bf_hi(v[i]) - c;
        s1 += a + b;
        s2 = __builtin_fmaf(a, a, __builtin_fmaf(b, b, s2));
    }
    wave_sum2(s1, s2);
    const float dm = s1 * (1.0f / D);
    const float var = __builtin_fmaxf(__builtin_fmaf(-dm, dm, s2 * (1.0f / D)), 0.f);
    m = rbf(c + dm);
    const float v2 = rbf(rbf(var) + 1e-5f);
    const float q0 = __builtin_amdgcn_sqrtf(v2);
    const float q1 = __builtin_fmaf(__builtin_fmaf(-q0, q0, v2), 0.5f * __builtin_amdgcn_rcpf(q0), q0);
    s = rbf(q1);
    rs = __builtin_amdgcn_rcpf(s);
}
DEV u32 ln_apply2(float x0, float x1, float m, float s, float rs) {
    const u32 a = pk_bf(x0 - m, x1 - m);
    const float a0 = bf_lo(a), a1 = bf_hi(a);
    const float q0 = a0 * rs, q1 = a1 * rs;
    return pk_bf(__builtin_fmaf(__builtin_fmaf(-q0, s, a0), rs, q0), __builtin_fmaf(__builtin_fmaf(-q1, s, a1), rs, q1));
}

template <int OFF>
DEV void wr8(u32 m0v, u32 a, u32 b, u32 c, u32 d, u32 e, u32 f, u32 g, u32 h) {
    asm volatile("s_mov_b32 m0, %8\n\ts_nop 0\n\t"
                 "ds_write_addtid_b32 %0 offset:%9\n\tds_write_addtid_b32 %1 offset:%10\n\t"
                 "ds_write_addtid_b32 %2 offset:%11\n\tds_write_addtid_b32 %3 offset:%12\n\t"
                 "ds_write_addtid_b32 %4 offset:%13\n\tds_write_addtid_b32 %5 offset:%14\n\t"
                 "ds_write_addtid_b32 %6 offset:%15\n\tds_write_addtid_b32 %7 offset:%16"
                 :: "v"(a), "v"(b), "v"(c), "v"(d), "v"(e), "v"(f), "v"(g), "v"(h), "s"(m0v), "i"(OFF), "i"(OFF + TROW),
                    "i"(OFF + 2 * TROW), "i"(OFF + 3 * TROW), "i"(OFF + 4 * TROW), "i"(OFF + 5 * TROW), "i"(OFF + 6 * TROW),
                    "i"(OFF + 7 * TROW) : "memory");
}
template <int OFF>
DEV void wr3(u32 m0v, u32 a, u32 b, u32 c) {
    asm volatile("s_mov_b32 m0, %3\n\ts_nop 0\n\t"
                 "ds_write_addtid_b32 %0 offset:%4\n\tds_write_addtid_b32 %1 offset:%5\n\tds_write_addtid_b32 %2 offset:%6"
                 :: "v"(a), "v"(b), "v"(c), "s"(m0v), "i"(OFF), "i"(OFF + TROW), "i"(OFF + 2 * TROW) : "memory");
}
DEV void lds_drain() { asm volatile("s_waitcnt lgkmcnt(0)" ::: "memory"); }
DEV v4u ld_sc1(__amdgpu_buffer_rsrc_t rs, int off) { return __builtin_amdgcn_raw_buffer_load_b128(rs, off, 0, 16); }
DEV void st_sc1(__amdgpu_buffer_rsrc_t rs, int off, v4u v) { __builtin_amdgcn_raw_buffer_store_b128(v, rs, off, 0, 16); }
DEV void drain() { asm volatile("s_waitcnt vmcnt(0)" ::: "memory"); }
DEV void vm_clean() { __builtin_amdgcn_s_waitcnt(0x0F70); }
DEV void cbar() { asm volatile("" ::: "memory"); }

constexpr u32 kidx(int lag) {
    if (lag < 0 || lag >= L) return 660u;
    const int sh = lag < 256 ? 0 : (lag < 512 ? 1 : (lag < 1024 ? 2 : 3));
    const int k = sh ? ((lag + (1 << (sh - 1)) - 1 + ((lag >> sh) & 1)) >> sh) : lag;
    return (u32)(128 * sh + k);
}
struct GatherTab { u32 v[66][8]; };
constexpr GatherTab make_gather_tab() {
    GatherTab t{};
    for (int i = 0; i < 66; ++i) {
        const int a = i < 65 ? 194 + i : 1;
        for (int k = 0; k < 6; ++k) t.v[i][k] = (kidx(Z0 - 8 * a - 2 * k) * 2) | (kidx(Z0 - 8 * a - 2 * k - 1) * 2 << 16);
    }
    return t;
}

template <int N0, int N1>
DEV void conv_load_frags(u32 kc, LAS const unsigned char* sm, int lane, h8 (&A)[29]) {
    const int q = lane & 15, g = lane >> 4;
    const int s0 = Z0 - q + 8 * g;
    const int sg = s0 & 3;
    u32 pl = kc + sg * KCS + 2 * (s0 - sg) - 64 * (N1 - 1);
    asm volatile("" : "+v"(pl));
#pragma unroll
    for (int n = N0; n < N1; ++n) {
        const v2u lo = *(LAS const v2u*)(sm + pl + 64 * (N1 - 1 - n));
        asm volatile("");
        const v2u hi = *(LAS const v2u*)(sm + pl + 64 * (N1 - 1 - n) + 8);
        asm volatile("");
        const v4u t = {lo.x, lo.y, hi.x, hi.y};
        A[n - N0] = __builtin_bit_cast(h8, t);
    }
}
template <int N0, int N1, int DEPTH>
DEV void conv_run(const h8 (&A)[29], LAS const unsigned char* ub_lo, f4 (&acc)[8]) {
    constexpr int NU_HI = N1 - 1 < 8 ? N1 - 1 : 8;
    constexpr int NU_LO = N0 - 56;
    constexpr int STEPS = NU_HI - NU_LO + 1;
    h8 B[DEPTH];
    constexpr int T0 = N0 <= 8 ? 0 : (N0 - 8 + 7) / 8;
#pragma unroll
    for (int T = T0; T < 8; ++T) asm volatile("" : "+v"(acc[T]));
    asm volatile("s_nop 4");
#pragma unroll
    for (int k = 0; k < DEPTH; ++k) B[k] = *(LAS const h8*)(ub_lo + 64 * (8 - (NU_HI - k)));
    __builtin_amdgcn_sched_barrier(0);
#pragma unroll
    for (int k = 0; k < STEPS; ++k) {
        const int nu = NU_HI - k;
#pragma unroll
        for (int T = 0; T < 8; ++T) {
            const int n = nu + 8 * T;
            if (n >= N0 && n < N1) asm volatile("v_mfma_f32_16x16x32_f16 %0, %1, %2, %0" : "+v"(acc[T]) : "v"(A[n - N0]), "v"(B[k % DEPTH]));
        }
        if (k + DEPTH < STEPS) B[k % DEPTH] = *(LAS const h8*)(ub_lo + 64 * (8 - (nu - DEPTH)));
        __builtin_amdgcn_sched_barrier(0);
    }
    asm volatile("s_nop 15\n\ts_nop 7");
#pragma unroll
    for (int T = T0; T < 8; ++T) asm volatile("" : "+v"(acc[T]));
}
DEV constexpr int pslot(int job, int T) { return job == 0 ? T : (job == 1 ? 7 + T : 11 + T); }

#define STAMP(i) do {} while (0)
#define PSTAMP(i) do {} while (0)
#define LSTAMP(i) STAMP(i)
#define STAMP_TID 0
}

__device__ const GatherTab gather_tab = make_gather_tab();

__global__ void __launch_bounds__(NT, 3)
s4d_moe_fused(const u16* __restrict__ x, const u16* __restrict__ pLogA, const u16* __restrict__ pAim,
              const u16* __restrict__ pCr, const u16* __restrict__ pCi, const u16* __restrict__ pLogDt,
              const u16* __restrict__ pDs, const u16* __restrict__ pWr, const u16* __restrict__ pWg,
              const u16* __restrict__ pWu, const u16* __restrict__ pWd, u16* __restrict__ out,
              unsigned char* __restrict__ ws) {
    __shared__ __attribute__((aligned(16))) unsigned char smem_[160 * 1024];
    LAS unsigned char* sm = (LAS unsigned char*)smem_;
    asm volatile("" :: "s"(x), "s"(pLogA), "s"(pAim), "s"(pCr), "s"(pCi), "s"(pLogDt), "s"(pDs), "s"(pWr), "s"(pWg), "s"(pWu), "s"(pWd),
                 "s"(out), "s"(ws));
    const int tid = threadIdx.x, lane = tid & 63;
    const int w = __builtin_amdgcn_readfirstlane(tid >> 6);
    const int b = blockIdx.x, grp = b & 7, slot = b >> 3;
    const bool is_conv = slot < 27;
    const int d0 = 80 * grp + 3 * slot;
    const int nch = is_conv ? (slot == 26 ? 2 : 3) : 0;
    const int f = (slot - 27) * 8 + grp;
    const __amdgpu_buffer_rsrc_t rws = __builtin_amdgcn_make_buffer_rsrc((void*)ws, 0, WS_END, 0x00020000);

    STAMP(0);
    if (tid == 0) { *(LAS u32*)(sm + M_CLR) = 0; *(LAS u32*)(sm + M_CCNT) = 0; *(LAS u32*)(sm + M_P1) = 0; *(LAS u32*)(sm + M_P3) = 0; }

    u32 xrow[5], wr[5];
    if (w < 8) {
        const u32* xr = (const u32*)(x + (size_t)(8 * b + w) * D);
#pragma unroll
        for (int i = 0; i < 5; ++i) xrow[i] = xr[lane + 64 * i];
    }
    __syncthreads();
    u32 epoch = __builtin_amdgcn_raw_buffer_load_b32(rws, WS_EPOCH, 0, 0);
    auto get_tag = [&]() { u32 t = epoch; asm volatile("" : "+v"(t)); return t + 1u; };

    auto p1_rows = [&]() {
        float m, s, rs;
        __builtin_amdgcn_s_setprio(3);
        ln_stats(xrow, m, s, rs);
#pragma unroll
        for (int i = 0; i < 5; ++i) {
            const u32 ub = ln_apply2(bf_lo(xrow[i]), bf_hi(xrow[i]), m, s, rs);
            *(LAS u32*)(sm + LD_UT + w * HT_ROW + (lane + 64 * i) * 4) = pk_h(bf_lo(ub), bf_hi(ub));
        }
        lds_drain();
        if (lane == 0) __hip_atomic_fetch_add((LAS u32*)(sm + M_P1), 1u, __ATOMIC_RELAXED, __HIP_MEMORY_SCOPE_WORKGROUP);
        __builtin_amdgcn_s_setprio(0);
        STAMP(1);
    };
    auto p1_store = [&]() {
        {
            {
                u32 sp = 0;
                while (__hip_atomic_load((LAS u32*)(sm + M_P1), __ATOMIC_RELAXED, __HIP_MEMORY_SCOPE_WORKGROUP) < 8u && ++sp < (1u << 22))
                    __builtin_amdgcn_s_sleep(1);
                cbar();
                PSTAMP(16);
            }
            auto tr = [&](int grp64) {
                const int cp = 64 * grp64 + (lane >> 1) + 32 * (lane & 1);
                u32 v[8];
#pragma unroll
                for (int k = 0; k < 8; ++k) v[k] = *(LAS const u32*)(sm + LD_UT + k * HT_ROW + cp * 4);
                const bool odd = lane & 1;
                u32 keep[4], recv[4];
#pragma unroll
                for (int k = 0; k < 4; ++k) {
                    const u32 lo = __builtin_amdgcn_perm(v[2 * k + 1], v[2 * k], 0x05040100u), hi = __builtin_amdgcn_perm(v[2 * k + 1], v[2 * k], 0x07060302u);
                    keep[k] = odd ? hi : lo;
                    recv[k] = dppu<0xB1>(odd ? lo : hi);
                }
                const v4u a = {odd ? recv[0] : keep[0], odd ? recv[1] : keep[1], odd ? recv[2] : keep[2], odd ? recv[3] : keep[3]};
                const v4u c = {odd ? keep[0] : recv[0], odd ? keep[1] : recv[1], odd ? keep[2] : recv[2], odd ? keep[3] : recv[3]};
                st_sc1(rws, WS_UX + b * (D * 16) + (128 * grp64 + lane) * 16, a);
                st_sc1(rws, WS_UX + b * (D * 16) + (128 * grp64 + 64 + lane) * 16, c);
            };
            tr(w - 9);
            if (w < 11) tr(w - 6);
            PSTAMP(18);
        }
    };
    auto p1_flag = [&]() {
        {
            drain();
            PSTAMP(19);
            u32 old = 0;
            if (lane == 0) old = __hip_atomic_fetch_add((LAS u32*)(sm + M_CLR), 1u, __ATOMIC_RELAXED, __HIP_MEMORY_SCOPE_WORKGROUP);
            old = __builtin_amdgcn_readfirstlane(old);
            if (old == 2u && lane < 8) __builtin_amdgcn_raw_buffer_store_b32(get_tag(), rws, WS_G1 + lane * 1024 + b * 4, 0, 16);
        }
    };
    auto clear_slots = [&]() {
        const v4u sv = {SENT, SENT, SENT, SENT};
        const int t = tid - 576;
#pragma unroll
        for (int i = 0; i < 4; ++i) st_sc1(rws, WS_HX + b * HXB + (t + 192 * i) * 16, sv);
        if (t < 96) st_sc1(rws, WS_HX + b * HXB + (768 + t) * 16, sv);
        if (w == 10) {
            if (lane >= 32 && lane < 36) st_sc1(rws, WS_G3 + b * 64 + (lane - 32) * 16, sv);
            if (!is_conv && f < 8 && lane >= 36 && lane < 38) st_sc1(rws, WS_IDX + f * 256 + (lane - 36) * 16, sv);
        }
        if (w == 11 && !is_conv && f < 24) st_sc1(rws, WS_ACT + (lane >> 3) * (F * 2) + f * 128 + (lane & 7) * 16, sv);
    };
    auto clear_rows = [&]() {
        const v4u sv = {SENT, SENT, SENT, SENT};
        const int t = tid - 576;
#pragma unroll
        for (int i = 0; i < 3; ++i) st_sc1(rws, WS_HROW + b * (D * 16) + (t + 192 * i) * 16, sv);
        if (w == 9) st_sc1(rws, WS_HROW + b * (D * 16) + (576 + lane) * 16, sv);
        drain();
    };
    if (w >= 9) clear_slots();

    v4u wf[20];
    if (is_conv) {
        u16 pla[3] = {0, 0, 0}, pai[3] = {0, 0, 0}, pcr[3] = {0, 0, 0}, pci[3] = {0, 0, 0};
        const int ad = min(d0 + (w < 9 ? w / 3 : w - 9), D - 1);
        const u16 pdt = pLogDt[ad];
        if (w < 9) {
            const int n = (w % 3) * 64 + lane;
            pla[0] = pLogA[ad * NS + n];
            pai[0] = pAim[ad * NS + n];
        } else {
#pragma unroll
            for (int i = 0; i < 3; ++i) {
                pla[i] = pLogA[ad * NS + i * 64 + lane];
                pai[i] = pAim[ad * NS + i * 64 + lane];
                pcr[i] = pCr[ad * NS + i * 64 + lane];
                pci[i] = pCi[ad * NS + i * 64 + lane];
            }
        }
        const int eo = tid / 3, ec = tid - 3 * eo;
        const u16 pdsk = pDs[min(d0 + min(tid & 3, 2), D - 1)];
        u32 gi[6], gi2[6];
        {
            const v4u* gt = (const v4u*)gather_tab.v[lane];
            const v4u g0 = gt[0], g1 = gt[1];
            gi[0] = g0.x; gi[1] = g0.y; gi[2] = g0.z; gi[3] = g0.w; gi[4] = g1.x; gi[5] = g1.y;
            const v4u* gu = (const v4u*)gather_tab.v[64 + (lane & 1)];
            const v4u g2 = gu[0], g3 = gu[1];
            gi2[0] = g2.x; gi2[1] = g2.y; gi2[2] = g2.z; gi2[3] = g2.w; gi2[4] = g3.x; gi2[5] = g3.y;
        }
        if (w < 8) p1_rows();
        if (w < 9) {
            const int c = w / 3;
            const u32 mb = LD_TBL + c * TCH + (w % 3) * 256;
            const float dt = rbf(__expf(bf2f(pdt)));
            const float zr = -rbf(__expf(bf2f(pla[0]))) * dt;
            const float zi = -bf2f(pai[0]) * dt;
            const cf B1 = cexp_pos(zr, zi, 1.f), H1 = cexp_pos(zr, zi, 128.f);
            const cf B2 = csq(B1), B3 = cmul(B2, B1), B4 = csq(B2), B5 = cmul(B4, B1), B6 = csq(B3), B7 = cmul(B4, B3);
            u32 hb[8];
            hb[0] = 0x00003C00u;
            hb[1] = c2h(B1); hb[2] = c2h(B2); hb[3] = c2h(B3); hb[4] = c2h(B4); hb[5] = c2h(B5); hb[6] = c2h(B6); hb[7] = c2h(B7);
            wr8<16 * TROW>(mb, hb[0], hb[1], hb[2], hb[3], hb[4], hb[5], hb[6], hb[7]);
            const cf H2 = csq(H1), H3 = cmul(H2, H1), H4 = csq(H2), H5 = cmul(H4, H1), H6 = csq(H3), H7 = cmul(H4, H3), H8 = csq(H4);
            const u32 h1 = c2h(H1), h2_ = c2h(H2), h3 = c2h(H3), h4 = c2h(H4), h5 = c2h(H5), h6 = c2h(H6), h7 = c2h(H7);
            wr3<24 * TROW>(mb, h1, hcmul(h1, hb[1]), hcmul(h1, hb[2]));
            const u32 mh = mb + 26 * TROW;
            wr8<1 * TROW>(mh, hcmul(h1, hb[3]), hcmul(h1, hb[4]), hcmul(h1, hb[5]), hcmul(h1, hb[6]), hcmul(h1, hb[7]),
                          h2_, hcmul(h2_, hb[2]), hcmul(h2_, hb[4]));
            wr8<9 * TROW>(mh, hcmul(h2_, hb[6]), h3, hcmul(h3, hb[2]), hcmul(h3, hb[4]), hcmul(h3, hb[6]),
                          h4, hcmul(h4, hb[4]), h5);
            wr8<17 * TROW>(mh, hcmul(h5, hb[4]), h6, hcmul(h6, hb[4]), h7, hcmul(h7, hb[4]), c2h(H8), c2h(cmul(H8, H1)),
                           c2h(csq(H5)));
            wr3<25 * TROW>(mh, c2h(cmul(H8, H3)), c2h(csq(H6)), c2h(cmul(H8, H5)));
            LAS u32* T = (LAS u32*)(sm + mb + lane * 4);
            T[54 * (TROW / 4)] = c2h(csq(H7));
            T[55 * (TROW / 4)] = c2h(cmul(H8, H7));
            T[56 * (TROW / 4)] = c2h(csq(H8));
            lds_drain();
        } else {
            __builtin_amdgcn_s_setprio(3);
            p1_store();
            p1_flag();
            __builtin_amdgcn_sched_barrier(0);
            const int c = w - 9;
            const float dt = rbf(__expf(bf2f(pdt)));
            float sabs = 0.f;
#pragma unroll
            for (int i = 0; i < 3; ++i) sabs += __builtin_fabsf(bf2f(pcr[i]) * dt) + __builtin_fabsf(bf2f(pci[i]) * dt);
            int e = 14 - __builtin_amdgcn_frexp_expf(wave_sum(sabs));
            e = e > 100 ? 100 : (e < -100 ? -100 : e);
            if (lane == 0) *(LAS int*)(sm + M_SCALE + 4 * c) = e;
            const float sc = __builtin_ldexpf(dt, e + 1);
            cf be[3], w8[3];
#pragma unroll
            for (int i = 0; i < 3; ++i) {
                w8[i] = cexp_pos(-rbf(__expf(bf2f(pla[i]))) * dt, bf2f(pai[i]) * dt, 8.f);
                be[i] = f2{bf2f(pcr[i]) * sc, bf2f(pci[i]) * sc};
            }
#pragma unroll
            for (int h = 0; h < 2; ++h) {
                u32 v[3][8];
#pragma unroll
                for (int j = 0; j < 8; ++j)
#pragma unroll
                    for (int i = 0; i < 3; ++i) {
                        v[i][j] = c2h(be[i]);
                        if (h == 0 || j < 7) be[i] = cmul(be[i], w8[i]);
                    }
#pragma unroll
                for (int i = 0; i < 3; ++i) {
                    const u32 mb = LD_TBL + c * TCH + i * 256;
                    if (h == 0) wr8<0>(mb, v[i][0], v[i][1], v[i][2], v[i][3], v[i][4], v[i][5], v[i][6], v[i][7]);
                    else wr8<8 * TROW>(mb, v[i][0], v[i][1], v[i][2], v[i][3], v[i][4], v[i][5], v[i][6], v[i][7]);
                }
            }
            if (lane < 16) *(LAS u16*)(sm + LD_KD + c * (KD_LEN * 2) + (656 + lane) * 2) = 0;
            lds_drain();
            __builtin_amdgcn_s_setprio(0);
        }
        STAMP(2);
        __syncthreads();
        STAMP(3);
        if (w == 11) {
            const u32 tag = get_tag();
            __builtin_amdgcn_s_sleep(E1_DELAY);
            u32 sp = 0;
            while (true) {
                cbar();
                const v4u fl = ld_sc1(rws, WS_G1 + (b & 7) * 1024 + lane * 16);
                if (__all((fl.x == tag) & (fl.y == tag) & (fl.z == tag) & (fl.w == tag)) || ++sp > SPIN_CAP) break;
                __builtin_amdgcn_s_sleep(2);
            }
        }
        if (w < 9) {
            const int c = w / 3, tile = w % 3;
            const int j = lane & 15, g = lane >> 4;
            int row = 16 + 16 * tile + j;
            if (row > 56) row = 56;
            u32 pa = LD_TBL + c * TCH + row * TROW + g * 16, pb = LD_TBL + c * TCH + j * TROW + g * 16;
            asm volatile("" : "+v"(pa), "+v"(pb));
            h8 fa[12], fb[12];
#pragma unroll
            for (int t = 0; t < 12; ++t) {
                fa[t] = *(LAS const h8*)(sm + pa + 64 * t);
                fb[t] = *(LAS const h8*)(sm + pb + 64 * t);
            }
            __builtin_amdgcn_sched_barrier(0);
            f4 acc0 = {0.f, 0.f, 0.f, 0.f}, acc1 = {0.f, 0.f, 0.f, 0.f};
#pragma unroll
            for (int t = 0; t < 12; t += 2) {
                acc0 = __builtin_amdgcn_mfma_f32_16x16x32_f16(fa[t], fb[t], acc0, 0, 0, 0);
                acc1 = __builtin_amdgcn_mfma_f32_16x16x32_f16(fa[t + 1], fb[t + 1], acc1, 0, 0, 0);
            }
            const f4 acc = acc0 + acc1;
            const u32 p01 = pk_h(acc[0], acc[1]), p23 = pk_h(acc[2], acc[3]);
            LAS unsigned char* kd = sm + LD_KD + c * (KD_LEN * 2);
            if (tile == 0) {
                *(LAS v2u*)(kd + 2 * (128 * (g >> 1) + 4 * (g & 1) + 8 * j)) = v2u{p01, p23};
            } else if (tile == 1) {
                if (g < 2) {
                    *(LAS v2u*)(kd + 2 * (256 + 64 * g + 4 * j)) = v2u{p01, p23};
                } else {
                    *(LAS u32*)(kd + 2 * (384 + 64 * (g - 2) + 2 * j)) = p01;
                    *(LAS u32*)(kd + 2 * (384 + 64 * (g - 2) + 32 + 2 * j)) = p23;
                }
            } else {
                const int r0 = 4 * g;
                if (r0 <= 8) *(LAS u16*)(kd + 2 * (512 + 16 * r0 + j)) = (u16)p01;
                if (r0 + 3 <= 8) {
                    *(LAS u16*)(kd + 2 * (512 + 16 * (r0 + 1) + j)) = (u16)(p01 >> 16);
                    *(LAS u16*)(kd + 2 * (512 + 16 * (r0 + 2) + j)) = (u16)p23;
                    *(LAS u16*)(kd + 2 * (512 + 16 * (r0 + 3) + j)) = (u16)(p23 >> 16);
                }
            }
        }
        __syncthreads();
        cbar();
        const v4u uv = ld_sc1(rws, WS_UX + eo * (D * 16) + min(d0 + ec, D - 1) * 16);
        STAMP(4);
        {
            auto gather = [&](int c, const u32 (&ix)[6], u32 (&P)[6]) {
                LAS const unsigned char* kd = sm + LD_KD + c * (KD_LEN * 2);
#pragma unroll
                for (int k = 0; k < 6; ++k)
                    P[k] = (u32) * (LAS const u16*)(kd + (ix[k] & 0xffffu)) | ((u32) * (LAS const u16*)(kd + (ix[k] >> 16)) << 16);
            };
            auto put = [&](int c, int a, const u32 (&P)[6]) {
                LAS unsigned char* dst = sm + LD_KC + c * (4 * KCS) + a * 16;
                *(LAS v4u*)(dst) = v4u{P[0], P[1], P[2], P[3]};
                *(LAS v4u*)(dst + KCS) = v4u{__builtin_amdgcn_alignbit(P[1], P[0], 16), __builtin_amdgcn_alignbit(P[2], P[1], 16),
                                             __builtin_amdgcn_alignbit(P[3], P[2], 16), __builtin_amdgcn_alignbit(P[4], P[3], 16)};
                *(LAS v4u*)(dst + 2 * KCS) = v4u{P[1], P[2], P[3], P[4]};
                *(LAS v4u*)(dst + 3 * KCS) = v4u{__builtin_amdgcn_alignbit(P[2], P[1], 16), __builtin_amdgcn_alignbit(P[3], P[2], 16),
                                                 __builtin_amdgcn_alignbit(P[4], P[3], 16), __builtin_amdgcn_alignbit(P[5], P[4], 16)};
            };
            if (w < 8) {
#pragma unroll
                for (int i = 0; i < 5; ++i) *(LAS u32*)(sm + LD_XR + w * 1280 + (lane + 64 * i) * 4) = xrow[i];
            }
            const int cc = tid >> 8, ca = 2 + (tid & 255), kind = w & 3;
            LAS const unsigned char* kd = sm + LD_KD + cc * (KD_LEN * 2);
            u32 P[6], Q[6];
            if (kind < 2) {
                const int c0 = 642 - ca;
                const u32 v0 = *(LAS const u16*)(kd + 2 * c0), v1 = *(LAS const u16*)(kd + 2 * c0 - 2);
                const u32 w00 = v0 | (v0 << 16), w11 = v1 | (v1 << 16);
                P[0] = ca == 2 ? (v0 << 16) : w00;
                P[1] = w00;
                P[2] = (ca & 1) ? w11 : (v0 | (v1 << 16));
                P[3] = w11; P[4] = w11; P[5] = w11;
            } else if (kind == 2) {
                const int c0 = 772 - 2 * ca;
                const u32 hi = *(LAS const u32*)(kd + 2 * c0), lo = *(LAS const u32*)(kd + 2 * c0 - 4);
                P[0] = __builtin_amdgcn_perm(hi, hi, 0x01000100u);
                P[1] = __builtin_amdgcn_perm(hi, lo, 0x03020504u);
                P[2] = __builtin_amdgcn_perm(lo, lo, 0x03020302u);
                P[3] = __builtin_amdgcn_perm(lo, lo, 0x01000100u);
                P[4] = P[3]; P[5] = P[3];
            } else {
                gather(cc, gi, P);
            }
            if (w == 11) gather((lane >> 1) % 3, gi2, Q);
            put(cc, ca, P);
            if (w == 11) {
                if (lane < 6) put(lane >> 1, (lane & 1) ? 1 : 258, Q);
            } else if (w == 10) {
                for (int i = lane; i < 72; i += 64) {
                    const int cs = i / 6, k = i % 6;
                    *(LAS v4u*)(sm + LD_KC + (cs >> 2) * (4 * KCS) + (cs & 3) * KCS + (k == 0 ? 0 : 258 + k) * 16) = v4u{0, 0, 0, 0};
                }
            } else if (w == 9) {
                for (int i = lane; i < 102; i += 64) {
                    const int c = i / 34, k = i % 34;
                    const int el = k < 32 ? 8 * k : 2304 + 8 * (k - 32);
                    *(LAS v4u*)(sm + LD_UU + c * (ULEN * 2) + el * 2) = v4u{0, 0, 0, 0};
                }
            }
        }
        __syncthreads();
        STAMP(5);
        int job = -1, jc = 0;
        if ((w & 3) == 0) { job = 2; jc = w >> 2; }
        else if (w < 8) { job = (w >> 2); jc = (w & 3) - 1; }
        h8 A[29];
        const u32 kc = LD_KC + jc * (4 * KCS);
        if (job == 0) conv_load_frags<0, 16>(kc, sm, lane, A);
        else if (job == 1) conv_load_frags<16, 36>(kc, sm, lane, A);
        else if (job == 2) conv_load_frags<36, 65>(kc, sm, lane, A);
        STAMP(6);
        *(LAS v4u*)(sm + LD_UU + ec * (ULEN * 2) + (256 + 8 * eo) * 2) = uv;
        STAMP(7);
        __syncthreads();
        STAMP(8);
        if (job >= 0) {
            f4 acc[8];
#pragma unroll
            for (int T = 0; T < 8; ++T) acc[T] = f4{0.f, 0.f, 0.f, 0.f};
            const int p = lane & 15, g = lane >> 4;
            u32 uba = LD_UU + jc * (ULEN * 2) + (16 * p + 8 * g) * 2;
            asm volatile("" : "+v"(uba));
            LAS const unsigned char* ub = sm + uba;
            LAS unsigned char* part = sm + LD_PART + jc * PT_CH + g * PT_G + p * 16;
            if (job == 0) {
                conv_run<0, 16, DEP_L>(A, ub, acc);
#pragma unroll
                for (int T = 0; T < 8; ++T) *(LAS f4*)(part + pslot(0, T) * PT) = acc[T];
            } else if (job == 1) {
                conv_run<16, 36, DEP_M>(A, ub, acc);
#pragma unroll
                for (int T = 1; T < 8; ++T) *(LAS f4*)(part + pslot(1, T) * PT) = acc[T];
            } else {
                conv_run<36, 65, DEP_H>(A, ub, acc);
#pragma unroll
                for (int T = 4; T < 8; ++T) *(LAS f4*)(part + pslot(2, T) * PT) = acc[T];
            }
        } else {
            clear_rows();
        }
        STAMP(9);
        __syncthreads();
        STAMP(10);
        {
            const float dsk = bf2f(pdsk);
            const int pc = tid & 3, cc = pc < 3 ? pc : 2;
            const int e = *(LAS const int*)(sm + M_SCALE + 4 * cc);
            const float isc = __builtin_ldexpf(1.f, -e);
            auto piece = [&](int eo_, float (&y)[8]) {
                const int T = eo_ >> 5, p = (eo_ >> 1) & 15, g0 = (eo_ & 1) * 2;
                LAS const unsigned char* part = sm + LD_PART + cc * PT_CH + g0 * PT_G + p * 16;
                const int sM = T >= 1 ? pslot(1, T) : 0, sH = T >= 4 ? pslot(2, T) : 0;
                const float kM = T >= 1 ? 1.f : 0.f, kH = T >= 4 ? 1.f : 0.f;
                const f4 l0 = *(LAS const f4*)(part + T * PT), l1 = *(LAS const f4*)(part + T * PT + PT_G);
                const f4 m0 = *(LAS const f4*)(part + sM * PT), m1 = *(LAS const f4*)(part + sM * PT + PT_G);
                const f4 h0 = *(LAS const f4*)(part + sH * PT), h1 = *(LAS const f4*)(part + sH * PT + PT_G);
                const v4u ur = *(LAS const v4u*)(sm + LD_UU + cc * (ULEN * 2) + (256 + 8 * eo_) * 2);
                const f4 y0 = (l0 + m0 * kM + h0 * kH) * isc, y1 = (l1 + m1 * kM + h1 * kH) * isc;
#pragma unroll
                for (int r = 0; r < 4; ++r) { y[r] = y0[r]; y[4 + r] = y1[r]; }
                const u32 uu[4] = {ur.x, ur.y, ur.z, ur.w};
                u32 o[4];
#pragma unroll
                for (int k = 0; k < 4; ++k) {
                    const h2 u2 = as_h2(uu[k]);
                    const u32 yb = pk_bf(y[2 * k], y[2 * k + 1]), ud = pk_bf((float)u2.x * dsk, (float)u2.y * dsk);
                    o[k] = pk_bf(bf_lo(yb) + bf_lo(ud), bf_hi(yb) + bf_hi(ud));
                }
                const bool real = pc < nch;
                return v4u{real ? o[0] : 0u, real ? o[1] : 0u, real ? o[2] : 0u, real ? o[3] : 0u};
            };
            const int pid = grp * 27 + slot;
            float ya[8], yb_[8];
            const int o1 = tid >> 2, o2 = 192 + ((tid - 512) >> 2);
            const v4u va = piece(o1, ya);
            v4u vb;
            if (w >= 8) vb = piece(o2, yb_);
            __builtin_amdgcn_raw_buffer_store_b128(va, rws, WS_HX + o1 * HXB + pid * 64 + pc * 16, 0, HX_AUX);
            if (w >= 8) __builtin_amdgcn_raw_buffer_store_b128(vb, rws, WS_HX + o2 * HXB + pid * 64 + pc * 16, 0, HX_AUX);
            STAMP(11);
            drain();
        }
    } else {
        v4u rw[16];
        auto load_weights = [&]() {
            if (f < 24) {
#pragma unroll
                for (int i = 0; i < 7; ++i) {
                    const int p = min(tid + NT * i, 5119);
                    rw[i] = *(const v4u*)(pWg + (size_t)(p >> 3) * F + 64 * f + 8 * (p & 7));
                    rw[8 + i] = *(const v4u*)(pWu + (size_t)(p >> 3) * F + 64 * f + 8 * (p & 7));
                }
            } else if (f < 34) {
#pragma unroll
                for (int i = 0; i < 16; ++i) {
                    const int p = tid + NT * i;
                    rw[i] = *(const v4u*)(pWd + (size_t)(p >> 3) * D + 64 * (f - 24) + 8 * (p & 7));
                }
            }
        };
        if (w < 8) p1_rows();
        if (w >= 9) { __builtin_amdgcn_s_setprio(3); p1_store(); p1_flag(); __builtin_amdgcn_s_setprio(0); }
        load_weights();
        const int g = lane >> 4, q = (lane & 15) >> 2, pp = lane & 3;
        if (f < 24) {
#pragma unroll
            for (int rnd = 0; rnd < 2; ++rnd) {
#pragma unroll
                for (int i = 0; i < 7; ++i) {
                    const int p = tid + NT * i;
                    if (p < 5120) *(LAS v4u*)(sm + LF_W + p * 16) = rw[8 * rnd + i];
                }
                __syncthreads();
                if (w < 8 && (w >> 2) == rnd) {
                    LAS const unsigned char* base = sm + LF_W + (w & 3) * 32 + pp * 8;
#pragma unroll
                    for (int s = 0; s < 20; ++s) {
                        const int k0 = 32 * s + 8 * g + q;
                        const s4 a = __builtin_amdgcn_ds_read_tr16_b64_v4i16((LAS s4*)(base + k0 * 128));
                        const s4 c = __builtin_amdgcn_ds_read_tr16_b64_v4i16((LAS s4*)(base + (k0 + 4) * 128));
                        const v2u a2 = __builtin_bit_cast(v2u, a), c2 = __builtin_bit_cast(v2u, c);
                        wf[s] = v4u{a2.x, a2.y, c2.x, c2.y};
                    }
                }
                __syncthreads();
            }
        } else if (f < 34) {
#pragma unroll
            for (int rnd = 0; rnd < 2; ++rnd) {
#pragma unroll
                for (int i = 0; i < 8; ++i) *(LAS v4u*)(sm + LF_W + (tid + NT * i) * 16) = rw[8 * rnd + i];
                __syncthreads();
                LAS const unsigned char* base = sm + LF_W + (w & 3) * 32 + pp * 8;
#pragma unroll
                for (int s = 0; s < 16; ++s) {
                    const int ks = 16 * (w >> 2) + s;
                    if ((ks >= 24) == (rnd == 1)) {
                        const int k0 = 32 * (ks - 24 * rnd) + 8 * g + q;
                        const s4 a = __builtin_amdgcn_ds_read_tr16_b64_v4i16((LAS s4*)(base + k0 * 128));
                        const s4 c = __builtin_amdgcn_ds_read_tr16_b64_v4i16((LAS s4*)(base + (k0 + 4) * 128));
                        const v2u a2 = __builtin_bit_cast(v2u, a), c2 = __builtin_bit_cast(v2u, c);
                        wf[s] = v4u{a2.x, a2.y, c2.x, c2.y};
                    }
                }
                __syncthreads();
            }
        }
        if (w >= 9) clear_rows();
        if (w < 8) {
#pragma unroll
            for (int i = 0; i < 5; ++i) *(LAS u32*)(sm + LD_XR + w * 1280 + (lane + 64 * i) * 4) = xrow[i];
        }
        __syncthreads();
        if (w == 0) {
            u32 sp = 0;
            while (true) {
                cbar();
                const u32 v = __builtin_amdgcn_raw_buffer_load_b32(rws, WS_HX + b * HXB, 0, 16);
                if (v != SENT || ++sp > SPIN_CAP) break;
                __builtin_amdgcn_s_sleep(FFN_IDLE);
            }
        }
    }
    if (w < 8) {
#pragma unroll
        for (int i = 0; i < 5; ++i) wr[i] = ((const u32*)pWr)[lane + 64 * i];
    }
    vm_clean();
    __syncthreads();
    STAMP(12);

    if (tid < D) {
        v4u v;
        u32 sp = 0;
        const int cg = tid / 80, cr = tid - 80 * cg, cs = cr / 3;
        const int hxo = WS_HX + b * HXB + (cg * 27 + cs) * 64 + (cr - 3 * cs) * 16;
        if (P3_DELAY && is_conv) __builtin_amdgcn_s_sleep(P3_DELAY);
        while (true) {
            cbar();
            v = ld_sc1(rws, hxo);
            const bool ok = (v.x != SENT) & (v.y != SENT) & (v.z != SENT) & (v.w != SENT);
            if (__all(ok) || ++sp > SPIN_CAP) break;
            if (P3_BACK) __builtin_amdgcn_s_sleep(P3_BACK);
        }
        STAMP(13);
        const bool odd = tid & 1;
        const u32 s0 = odd ? v.x : v.z, s1 = odd ? v.y : v.w;
        const u32 r0 = dppu<0xB1>(s0), r1 = dppu<0xB1>(s1);
        const u32 k0 = odd ? v.z : v.x, k1 = odd ? v.w : v.y;
        const u32 lo0 = odd ? r0 : k0, hi0 = odd ? k0 : r0, lo1 = odd ? r1 : k1, hi1 = odd ? k1 : r1;
        LAS u32* dst = (LAS u32*)(sm + LD_HT + (odd ? 4 * HT_ROW : 0) + (tid >> 1) * 4);
        dst[0 * (HT_ROW / 4)] = __builtin_amdgcn_perm(hi0, lo0, 0x05040100u);
        dst[1 * (HT_ROW / 4)] = __builtin_amdgcn_perm(hi0, lo0, 0x07060302u);
        dst[2 * (HT_ROW / 4)] = __builtin_amdgcn_perm(hi1, lo1, 0x05040100u);
        dst[3 * (HT_ROW / 4)] = __builtin_amdgcn_perm(hi1, lo1, 0x07060302u);
    }
    __syncthreads();
    u32 orow[5];
    const int mytok = 8 * b + w;
    if (w < 8) {
        u32 hv[5];
#pragma unroll
        for (int i = 0; i < 5; ++i) {
            const u32 a = *(LAS const u32*)(sm + LD_HT + w * HT_ROW + (lane + 64 * i) * 4);
            const u32 xv = *(LAS const u32*)(sm + LD_XR + w * 1280 + (lane + 64 * i) * 4);
            hv[i] = pk_bf(bf_lo(xv) + bf_lo(a), bf_hi(xv) + bf_hi(a));
        }
        float m, s, rs;
        ln_stats(hv, m, s, rs);
        float racc = 0.f;
#pragma unroll
        for (int i = 0; i < 5; ++i) {
            const u32 z = ln_apply2(bf_lo(hv[i]), bf_hi(hv[i]), m, s, rs);
            racc = __builtin_fmaf(bf_lo(z), bf_lo(wr[i]), __builtin_fmaf(bf_hi(z), bf_hi(wr[i]), racc));
            orow[i] = pk_bf(bf_lo(hv[i]) + bf_lo(z), bf_hi(hv[i]) + bf_hi(z));
        }
        const float r = wave_sum(racc);
        u32 old = 0;
        if (lane == 0) {
            *(LAS v2u*)(sm + M_G3 + w * 8) = v2u{(__float_as_uint(m) >> 16) | (__float_as_uint(s) & 0xffff0000u), pk_bf(r, 0.f) & 0xffffu};
            lds_drain();
            old = __hip_atomic_fetch_add((LAS u32*)(sm + M_P3), 1u, __ATOMIC_RELAXED, __HIP_MEMORY_SCOPE_WORKGROUP);
        }
        old = __builtin_amdgcn_readfirstlane(old);
        if (old == 7u && lane < 4) {
            cbar();
            st_sc1(rws, WS_G3 + b * 64 + lane * 16, *(LAS const v4u*)(sm + M_G3 + lane * 16));
        }
        STAMP(14);
    } else {
        if (HROW_DELAY) __builtin_amdgcn_s_sleep(HROW_DELAY);
        const int t = tid - 512;
        v4u a[3], xv[3];
#pragma unroll
        for (int i = 0; i < 3; ++i) {
            const int p = min(t + 256 * i, 639), k = p / 80, ch = p - 80 * k;
            a[i] = *(LAS const v4u*)(sm + LD_HT + k * HT_ROW + ch * 16);
            xv[i] = *(LAS const v4u*)(sm + LD_XR + k * 1280 + ch * 16);
        }
#pragma unroll
        for (int i = 0; i < 3; ++i) {
            const v4u h = {pk_bf(bf_lo(xv[i].x) + bf_lo(a[i].x), bf_hi(xv[i].x) + bf_hi(a[i].x)),
                           pk_bf(bf_lo(xv[i].y) + bf_lo(a[i].y), bf_hi(xv[i].y) + bf_hi(a[i].y)),
                           pk_bf(bf_lo(xv[i].z) + bf_lo(a[i].z), bf_hi(xv[i].z) + bf_hi(a[i].z)),
                           pk_bf(bf_lo(xv[i].w) + bf_lo(a[i].w), bf_hi(xv[i].w) + bf_hi(a[i].w))};
            if (i < 2 || w < 10) st_sc1(rws, WS_HROW + b * (D * 16) + (t + 256 * i) * 16, h);
        }
    }
    STAMP(15);

    if (!is_conv && f < 34) {
        u32 key[4], msv[4];
        if (w < 8) {
            __builtin_amdgcn_s_sleep(G3_DELAY);
            v4u g0, g1;
            u32 sp = 0;
            while (true) {
                cbar();
                g0 = ld_sc1(rws, WS_G3 + tid * 32);
                g1 = ld_sc1(rws, WS_G3 + tid * 32 + 16);
                const bool ok = (g0.y != SENT) & (g0.w != SENT) & (g1.y != SENT) & (g1.w != SENT);
                if (__all(ok) || ++sp > SPIN_CAP) break;
                if (G3_BACK) __builtin_amdgcn_s_sleep(G3_BACK);
            }
            LSTAMP(16);
            const u32 rb[4] = {g0.y, g0.w, g1.y, g1.w};
            msv[0] = g0.x; msv[1] = g0.z; msv[2] = g1.x; msv[3] = g1.z;
#pragma unroll
            for (int j = 0; j < 4; ++j) {
                const u32 o = (rb[j] & 0x8000u) ? (~rb[j] & 0xffffu) : (rb[j] | 0x8000u);
                key[j] = (o << 11) | (u32)(2047 - (4 * tid + j));
            }
            const u32 mx = wave_max(umax(umax(key[0], key[1]), umax(key[2], key[3])));
            if (lane == 0) *(LAS u32*)(sm + M_WMAX + 4 * w) = mx;
        }
        __syncthreads();
        if (w < 8) {
            const v4u a = *(LAS const v4u*)(sm + M_WMAX), c = *(LAS const v4u*)(sm + M_WMAX + 16);
            const u32 thr = min(min(min(a.x, a.y), min(a.z, a.w)), min(min(c.x, c.y), min(c.z, c.w)));
            u64 bal[4];
            int tot = 0;
#pragma unroll
            for (int j = 0; j < 4; ++j) { bal[j] = __ballot(key[j] >= thr); tot += __builtin_popcountll(bal[j]); }
            u32 base = 0;
            if (lane == 0) base = __hip_atomic_fetch_add((LAS u32*)(sm + M_CCNT), (u32)tot, __ATOMIC_RELAXED, __HIP_MEMORY_SCOPE_WORKGROUP);
            base = __builtin_amdgcn_readfirstlane(base);
#pragma unroll
            for (int j = 0; j < 4; ++j) {
                const u32 pos = base + __builtin_amdgcn_mbcnt_hi((u32)(bal[j] >> 32), __builtin_amdgcn_mbcnt_lo((u32)bal[j], 0));
                if (key[j] >= thr) *(LAS v2u*)(sm + LF_CAND + pos * 8) = v2u{key[j], msv[j]};
                base += __builtin_popcountll(bal[j]);
            }
        }
        __syncthreads();
        {
            const u32 cnt = *(LAS const u32*)(sm + M_CCNT);
            for (u32 i0 = 64 * w; i0 < cnt; i0 += NT) {
                const u32 i = i0 + lane;
                const v2u me = *(LAS const v2u*)(sm + LF_CAND + min(i, cnt - 1) * 8);
                u32 rank = 0;
                for (u32 j0 = 0; j0 < cnt; j0 += 64) {
                    const u32 ot = (j0 + lane) < cnt ? *(LAS const u32*)(sm + LF_CAND + (j0 + lane) * 8) : 0u;
                    const u32 nj = min(64u, cnt - j0);
                    for (u32 j = 0; j < nj; j += 4) {
                        rank += (u32)__builtin_amdgcn_readlane(ot, j) > me.x;
                        rank += (u32)__builtin_amdgcn_readlane(ot, j + 1) > me.x;
                        rank += (u32)__builtin_amdgcn_readlane(ot, j + 2) > me.x;
                        rank += (u32)__builtin_amdgcn_readlane(ot, j + 3) > me.x;
                    }
                }
                if (i < cnt && rank < 8) {
                    const u32 rbits = me.x >> 11;
                    const u32 rb = (rbits & 0x8000u) ? (rbits & 0x7fffu) : (~rbits & 0xffffu);
                    *(LAS v4u*)(sm + M_SEL + rank * 16) = v4u{2047u - (me.x & 2047u), me.y, rb, 0u};
                }
            }
        }
        __syncthreads();
        LSTAMP(17);
        if (tid < D) {
            const int k = tid / 80, ch = tid % 80;
            const v4u sel = *(LAS const v4u*)(sm + M_SEL + k * 16);
            v4u hv;
            u32 sp = 0;
            while (true) {
                cbar();
                hv = ld_sc1(rws, WS_HROW + (int)(sel.x & (L - 1)) * (D * 2) + ch * 16);
                const bool ok = (hv.x != SENT) & (hv.y != SENT) & (hv.z != SENT) & (hv.w != SENT);
                if (__all(ok) || ++sp > SPIN_CAP) break;
            }
            const float m = bf_lo(sel.y), s = bf_hi(sel.y), rs = __builtin_amdgcn_rcpf(s);
            const u32 hh[4] = {hv.x, hv.y, hv.z, hv.w};
            u32 zz[4];
#pragma unroll
            for (int i = 0; i < 4; ++i) zz[i] = ln_apply2(bf_lo(hh[i]), bf_hi(hh[i]), m, s, rs);
            *(LAS v4u*)(sm + LF_Z + k * HT_ROW + ch * 16) = v4u{zz[0], zz[1], zz[2], zz[3]};
            *(LAS v4u*)(sm + LF_H + k * HT_ROW + ch * 16) = hv;
        } else if (w == 11 && f < 8 && lane < 2) {
            const v4u s0 = *(LAS const v4u*)(sm + M_SEL + lane * 64), s1 = *(LAS const v4u*)(sm + M_SEL + lane * 64 + 16),
                      s2 = *(LAS const v4u*)(sm + M_SEL + lane * 64 + 32), s3 = *(LAS const v4u*)(sm + M_SEL + lane * 64 + 48);
            st_sc1(rws, WS_IDX + f * 256 + lane * 16, v4u{s0.x, s1.x, s2.x, s3.x});
        }
        __syncthreads();
        LSTAMP(18);
        const int row = lane & 7, g = lane >> 4;
        if (f < 24) {
            f4 acc = {0.f, 0.f, 0.f, 0.f};
            if (w < 8) {
                f4 acc1 = {0.f, 0.f, 0.f, 0.f};
                LAS const unsigned char* zb = sm + LF_Z + row * HT_ROW + 16 * g;
                b8 za[10];
#pragma unroll
                for (int s = 0; s < 10; ++s) za[s] = *(LAS const b8*)(zb + 64 * s);
                __builtin_amdgcn_sched_barrier(0);
#pragma unroll
                for (int s = 0; s < 20; s += 2) {
                    acc = __builtin_amdgcn_mfma_f32_16x16x32_bf16(za[s % 10], __builtin_bit_cast(b8, wf[s]), acc, 0, 0, 0);
                    acc1 = __builtin_amdgcn_mfma_f32_16x16x32_bf16(za[(s + 1) % 10], __builtin_bit_cast(b8, wf[s + 1]), acc1, 0, 0, 0);
                    if (s < 10) {
                        za[s] = *(LAS const b8*)(zb + 64 * (s + 10));
                        za[s + 1] = *(LAS const b8*)(zb + 64 * (s + 11));
                    }
                    __builtin_amdgcn_sched_barrier(0);
                }
                acc += acc1;
                if (w >= 4) *(LAS f4*)(sm + LF_RED + (w - 4) * 1024 + lane * 16) = acc;
            }
            __syncthreads();
            if (w < 4) {
                const f4 up = *(LAS const f4*)(sm + LF_RED + w * 1024 + lane * 16);
                float a[4];
#pragma unroll
                for (int r = 0; r < 4; ++r) {
                    const float gb = rbf(acc[r]);
                    const float sg = rbf(gb * __builtin_amdgcn_rcpf(1.f + __expf(-gb)));
                    a[r] = sg * rbf(up[r]);
                }
                if (g < 2) {
#pragma unroll
                    for (int r = 0; r < 4; r += 2) {
                        const u32 pr = pk_bf(a[r], a[r + 1]);
                        *(LAS u16*)(sm + LF_ACT + (4 * g + r) * 128 + (16 * w + (lane & 15)) * 2) = (u16)pr;
                        *(LAS u16*)(sm + LF_ACT + (4 * g + r + 1) * 128 + (16 * w + (lane & 15)) * 2) = (u16)(pr >> 16);
                    }
                }
                lds_drain();
                u32 old = 0;
                if (lane == 0) old = __hip_atomic_fetch_add((LAS u32*)(sm + M_P1), 1u, __ATOMIC_RELAXED, __HIP_MEMORY_SCOPE_WORKGROUP);
                old = __builtin_amdgcn_readfirstlane(old);
                if (old == 8u + 3u) {
                    cbar();
                    st_sc1(rws, WS_ACT + (lane >> 3) * (F * 2) + f * 128 + (lane & 7) * 16, *(LAS const v4u*)(sm + LF_ACT + lane * 16));
                }
            }
            LSTAMP(19);
        } else {
            {
                v4u a0, a1;
                u32 sp = 0;
                __builtin_amdgcn_s_sleep(ACT_DELAY);
                while (true) {
                    cbar();
                    a0 = ld_sc1(rws, WS_ACT + tid * 16);
                    a1 = ld_sc1(rws, WS_ACT + (tid + NT) * 16);
                    const bool ok = (a0.x != SENT) & (a0.y != SENT) & (a0.z != SENT) & (a0.w != SENT) & (a1.x != SENT) &
                                    (a1.y != SENT) & (a1.z != SENT) & (a1.w != SENT);
                    if (__all(ok) || ++sp > SPIN_CAP) break;
                    if (ACT_BACK) __builtin_amdgcn_s_sleep(ACT_BACK);
                }
                *(LAS v4u*)(sm + LF_ACT + (tid / 192) * ACT_ROW + (tid % 192) * 16) = a0;
                *(LAS v4u*)(sm + LF_ACT + ((tid + NT) / 192) * ACT_ROW + ((tid + NT) % 192) * 16) = a1;
            }
            __syncthreads();
            LSTAMP(20);
            f4 acc = {0.f, 0.f, 0.f, 0.f}, acc1 = {0.f, 0.f, 0.f, 0.f};
            LAS const unsigned char* ab = sm + LF_ACT + row * ACT_ROW + 1024 * (w >> 2) + 16 * g;
            b8 aa[16];
#pragma unroll
            for (int s = 0; s < 16; ++s) aa[s] = *(LAS const b8*)(ab + 64 * s);
            __builtin_amdgcn_sched_barrier(0);
#pragma unroll
            for (int s = 0; s < 16; s += 2) {
                acc = __builtin_amdgcn_mfma_f32_16x16x32_bf16(aa[s], __builtin_bit_cast(b8, wf[s]), acc, 0, 0, 0);
                acc1 = __builtin_amdgcn_mfma_f32_16x16x32_bf16(aa[s + 1], __builtin_bit_cast(b8, wf[s + 1]), acc1, 0, 0, 0);
            }
            acc += acc1;
            *(LAS f4*)(sm + LF_RED + w * 1024 + lane * 16) = acc;
            __syncthreads();
            if (tid < 512) {
                const int tok = tid >> 6, col = tid & 63, tile = col >> 4;
                const int off = ((tok >> 2) * 16 + (col & 15)) * 16 + (tok & 3) * 4;
                const float y = *(LAS const float*)(sm + LF_RED + tile * 1024 + off) + *(LAS const float*)(sm + LF_RED + (4 + tile) * 1024 + off) +
                                *(LAS const float*)(sm + LF_RED + (8 + tile) * 1024 + off);
                const v4u sel = *(LAS const v4u*)(sm + M_SEL + tok * 16);
                const int d = 64 * (f - 24) + col;
                const float z = bf2f(*(LAS const u16*)(sm + LF_Z + tok * HT_ROW + d * 2));
                const float h = bf2f(*(LAS const u16*)(sm + LF_H + tok * HT_ROW + d * 2));
                const float r = bf2f((u16)sel.z);
                const float sg = rbf(__builtin_amdgcn_rcpf(1.f + __expf(-r)));
                const float upd = rbf(rbf(y) * sg);
                out[(size_t)(sel.x & (L - 1)) * D + d] = (u16)pk_bf(h + rbf(z + upd), 0.f);
            }
        }
    }

    STAMP(21);
    if (w < 8) {
        v4u i0, i1;
        u32 sp = 0;
        while (true) {
            cbar();
            i0 = ld_sc1(rws, WS_IDX + (b & 7) * 256);
            i1 = ld_sc1(rws, WS_IDX + (b & 7) * 256 + 16);
            const bool ok = (i0.x != SENT) & (i0.y != SENT) & (i0.z != SENT) & (i0.w != SENT) & (i1.x != SENT) & (i1.y != SENT) &
                            (i1.z != SENT) & (i1.w != SENT);
            if (__all(ok) || ++sp > SPIN_CAP) break;
            __builtin_amdgcn_s_sleep(16);
        }
        const u32 t = (u32)mytok;
        const bool routed = (i0.x == t) | (i0.y == t) | (i0.z == t) | (i0.w == t) | (i1.x == t) | (i1.y == t) | (i1.z == t) | (i1.w == t);
        if (!routed) {
            const __amdgpu_buffer_rsrc_t ro = __builtin_amdgcn_make_buffer_rsrc((void*)out, 0, L * D * 2, 0x00020000);
#pragma unroll
            for (int i = 0; i < 5; ++i) __builtin_amdgcn_raw_buffer_store_b32(orow[i], ro, mytok * (D * 2) + (lane + 64 * i) * 4, 0, OUT_AUX);
        }
        if (b == 0 && tid == 0) *(u32*)(ws + WS_EPOCH) = get_tag();
    }
}

extern "C" void kernel_launch(void* const* d_in, const int* in_sizes, int n_in, void* d_out, int out_size, void* d_ws,
                              size_t ws_size, hipStream_t stream) {
    (void)in_sizes; (void)n_in; (void)out_size; (void)ws_size;
    s4d_moe_fused<<<256, NT, 0, stream>>>((const u16*)d_in[0], (const u16*)d_in[1], (const u16*)d_in[2], (const u16*)d_in[3],
                                          (const u16*)d_in[4], (const u16*)d_in[5], (const u16*)d_in[6],
                                          (const u16*)d_in[8], (const u16*)d_in[9], (const u16*)d_in[10],
                                          (const u16*)d_in[11], (u16*)d_out, (unsigned char*)d_ws);
}
```

```cpp
#define STAMPS
#define PRINTF
#include <hip/hip_runtime.h>
#include <stdio.h>
#include <stdint.h>

typedef unsigned int u32;
typedef unsigned long long u64;
typedef unsigned short u16;
typedef u32 v4u __attribute__((ext_vector_type(4)));
typedef u32 v2u __attribute__((ext_vector_type(2)));
typedef float f4 __attribute__((ext_vector_type(4)));
typedef float f2 __attribute__((ext_vector_type(2)));
typedef _Float16 h2 __attribute__((ext_vector_type(2)));
typedef _Float16 h8 __attribute__((ext_vector_type(8)));
typedef __bf16 b2 __attribute__((ext_vector_type(2)));
typedef __bf16 b8 __attribute__((ext_vector_type(8)));
typedef short s4 __attribute__((ext_vector_type(4)));
#define LAS __attribute__((address_space(3)))
#define DEV __device__ __forceinline__

namespace {

constexpr int L = 2048, D = 640, NS = 192, F = 1536;
constexpr int NT = 768;
constexpr u32 SENT = 0xFFFFFFFFu;
constexpr u32 SPIN_CAP = 1u << 16;
#define DEP_L 4
#define DEP_M 4
#define DEP_H 4
#define P3_DELAY 0
#define P3_BACK 0
#define G3_DELAY 8
#define G3_BACK 0
#define ACT_DELAY 8
#define ACT_BACK 0
#define OUT_AUX 0
#define FFN_IDLE 4
#define HX_AUX 16
#define HROW_DELAY 0
#define CODE_KB 32
#define E1_DELAY 12

constexpr int WS_EPOCH = 0;
constexpr int WS_G1 = 4096;
constexpr int WS_G3 = WS_G1 + 8192;
constexpr int WS_IDX = WS_G3 + 16384;
constexpr int WS_ACT = WS_IDX + 2048;
constexpr int WS_UX = 65536;
constexpr int HXB = 216 * 64;
constexpr int WS_HX = WS_UX + L * D * 2;
constexpr int WS_HROW = WS_HX + 256 * HXB;
constexpr int WS_END = WS_HROW + L * D * 2;
constexpr int WS_DBG = 16 << 20;

constexpr int TCH = 800;
constexpr int TROW = 3 * TCH;
constexpr int LD_TBL = 0;
constexpr int KD_LEN = 672;
constexpr int LD_KD = 136800;
constexpr int LD_UT = LD_KD + 3 * KD_LEN * 2;
constexpr int LD_MISC = 153600;
constexpr int KCS = 4416;
constexpr int LD_KC = 0;
constexpr int ULEN = 2320;
constexpr int PT_G = 320;
constexpr int PT = 4 * PT_G;
constexpr int PT_CH = 19 * PT + 48;
constexpr int LD_PART = 0;
constexpr int LD_UU = 99328;
constexpr int LD_XR = 114688;
constexpr int LD_HT = 0;
constexpr int HT_ROW = 648 * 2;
constexpr int M_CLR = LD_MISC;
constexpr int M_CCNT = LD_MISC + 4;
constexpr int M_P1 = LD_MISC + 8;
constexpr int M_P3 = LD_MISC + 12;
constexpr int M_G3 = LD_MISC + 256;
constexpr int M_SCALE = LD_MISC + 16;
constexpr int M_WMAX = LD_MISC + 64;
constexpr int M_SEL = LD_MISC + 128;
constexpr int M_STAMP = LD_MISC + 512;
constexpr int Z0 = 2064;

constexpr int LF_W = 0;
constexpr int LF_CAND = 16384;
constexpr int LF_Z = 32768;
constexpr int LF_H = LF_Z + 8 * HT_ROW;
constexpr int LF_RED = 57344;
constexpr int LF_ACT = 73728;
constexpr int ACT_ROW = 3088;

DEV float bf_lo(u32 v) { return __uint_as_float(v << 16); }
DEV float bf_hi(u32 v) { return __uint_as_float(v & 0xffff0000u); }
DEV float bf2f(u16 v) { return __uint_as_float((u32)v << 16); }
DEV u32 pk_bf(float a, float b) {
    f2 v = {a, b};
    return __builtin_bit_cast(u32, __builtin_convertvector(v, b2));
}
DEV h2 as_h2(u32 v) { return __builtin_bit_cast(h2, v); }
DEV float rbf(float a) { return bf_lo(pk_bf(a, a)); }
DEV u32 pk_h(float a, float b) {
    f2 v = {a, b};
    return __builtin_bit_cast(u32, __builtin_convertvector(v, h2));
}
template <int CTRL> DEV float dppf(float v) {
    return __int_as_float(__builtin_amdgcn_update_dpp(0, __float_as_int(v), CTRL, 0xf, 0xf, true));
}
template <int CTRL> DEV u32 dppu(u32 v) { return (u32)__builtin_amdgcn_update_dpp(0, (int)v, CTRL, 0xf, 0xf, true); }
DEV float wave_sum(float v) {
    v += dppf<0xB1>(v);
    v += dppf<0x4E>(v);
    v += dppf<0x141>(v);
    v += dppf<0x140>(v);
    auto r = __builtin_amdgcn_permlane16_swap(__float_as_uint(v), __float_as_uint(v), false, false);
    v = __uint_as_float(r[0]) + __uint_as_float(r[1]);
    auto q = __builtin_amdgcn_permlane32_swap(__float_as_uint(v), __float_as_uint(v), false, false);
    return __uint_as_float(q[0]) + __uint_as_float(q[1]);
}
DEV u32 umax(u32 a, u32 b) { return a > b ? a : b; }
DEV u32 wave_max(u32 v) {
    v = umax(v, dppu<0xB1>(v));
    v = umax(v, dppu<0x4E>(v));
    v = umax(v, dppu<0x141>(v));
    v = umax(v, dppu<0x140>(v));
    auto r = __builtin_amdgcn_permlane16_swap(v, v, false, false);
    v = umax(r[0], r[1]);
    auto q = __builtin_amdgcn_permlane32_swap(v, v, false, false);
    return umax(q[0], q[1]);
}

typedef f2 cf;
DEV cf cmul(cf a, cf b) { return __builtin_elementwise_fma(f2{a.y, a.y}, f2{-b.y, b.x}, f2{a.x, a.x} * b); }
DEV cf csq(cf a) { return cmul(a, a); }
DEV cf cexp_pos(float zr, float zi, float pos) {
    const float mag = __builtin_amdgcn_exp2f(zr * pos * 1.4426950408889634f);
    const float rev = __builtin_amdgcn_fractf(zi * pos * 0.15915494309189535f);
    return f2{mag * __builtin_amdgcn_cosf(rev), mag * __builtin_amdgcn_sinf(rev)};
}
DEV u32 c2h(cf a) { return pk_h(a.x, a.y); }
DEV u32 hcmul(u32 a_, u32 b_) {
    const h2 a = __builtin_bit_cast(h2, a_), b = __builtin_bit_cast(h2, b_);
    const h2 t = h2{a.x, a.x} * b;
    const h2 r = __builtin_elementwise_fma(h2{a.y, a.y}, h2{-b.y, b.x}, t);
    return __builtin_bit_cast(u32, r);
}

DEV void wave_sum2(float& a, float& b) {
    a += dppf<0xB1>(a);  b += dppf<0xB1>(b);
    a += dppf<0x4E>(a);  b += dppf<0x4E>(b);
    a += dppf<0x141>(a); b += dppf<0x141>(b);
    a += dppf<0x140>(a); b += dppf<0x140>(b);
    auto r = __builtin_amdgcn_permlane16_swap(__float_as_uint(a), __float_as_uint(a), false, false);
    auto t = __builtin_amdgcn_permlane16_swap(__float_as_uint(b), __float_as_uint(b), false, false);
    a = __uint_as_float(r[0]) + __uint_as_float(r[1]);
    b = __uint_as_float(t[0]) + __uint_as_float(t[1]);
    auto q = __builtin_amdgcn_permlane32_swap(__float_as_uint(a), __float_as_uint(a), false, false);
    auto u = __builtin_amdgcn_permlane32_swap(__float_as_uint(b), __float_as_uint(b), false, false);
    a = __uint_as_float(q[0]) + __uint_as_float(q[1]);
    b = __uint_as_float(u[0]) + __uint_as_float(u[1]);
}
DEV void ln_stats(const u32 (&v)[5], float& m, float& s, float& rs) {
    const float c = bf_lo(__builtin_amdgcn_readfirstlane(v[0]));
    float s1 = 0.f, s2 = 0.f;
#pragma unroll
    for (int i = 0; i < 5; ++i) {
        const float a = bf_lo(v[i]) - c, b = bf_hi(v[i]) - c;
        s1 += a + b;
        s2 = __builtin_fmaf(a, a, __builtin_fmaf(b, b, s2));
    }
    wave_sum2(s1, s2);
    const float dm = s1 * (1.0f / D);
    const float var = __builtin_fmaxf(__builtin_fmaf(-dm, dm, s2 * (1.0f / D)), 0.f);
    m = rbf(c + dm);
    const float v2 = rbf(rbf(var) + 1e-5f);
    const float q0 = __builtin_amdgcn_sqrtf(v2);
    const float q1 = __builtin_fmaf(__builtin_fmaf(-q0, q0, v2), 0.5f * __builtin_amdgcn_rcpf(q0), q0);
    s = rbf(q1);
    rs = __builtin_amdgcn_rcpf(s);
}
DEV u32 ln_apply2(float x0, float x1, float m, float s, float rs) {
    const u32 a = pk_bf(x0 - m, x1 - m);
    const float a0 = bf_lo(a), a1 = bf_hi(a);
    const float q0 = a0 * rs, q1 = a1 * rs;
    return pk_bf(__builtin_fmaf(__builtin_fmaf(-q0, s, a0), rs, q0), __builtin_fmaf(__builtin_fmaf(-q1, s, a1), rs, q1));
}

template <int OFF>
DEV void wr8(u32 m0v, u32 a, u32 b, u32 c, u32 d, u32 e, u32 f, u32 g, u32 h) {
    asm volatile("s_mov_b32 m0, %8\n\ts_nop 0\n\t"
                 "ds_write_addtid_b32 %0 offset:%9\n\tds_write_addtid_b32 %1 offset:%10\n\t"
                 "ds_write_addtid_b32 %2 offset:%11\n\tds_write_addtid_b32 %3 offset:%12\n\t"
                 "ds_write_addtid_b32 %4 offset:%13\n\tds_write_addtid_b32 %5 offset:%14\n\t"
                 "ds_write_addtid_b32 %6 offset:%15\n\tds_write_addtid_b32 %7 offset:%16"
                 :: "v"(a), "v"(b), "v"(c), "v"(d), "v"(e), "v"(f), "v"(g), "v"(h), "s"(m0v), "i"(OFF), "i"(OFF + TROW),
                    "i"(OFF + 2 * TROW), "i"(OFF + 3 * TROW), "i"(OFF + 4 * TROW), "i"(OFF + 5 * TROW), "i"(OFF + 6 * TROW),
                    "i"(OFF + 7 * TROW) : "memory");
}
template <int OFF>
DEV void wr3(u32 m0v, u32 a, u32 b, u32 c) {
    asm volatile("s_mov_b32 m0, %3\n\ts_nop 0\n\t"
                 "ds_write_addtid_b32 %0 offset:%4\n\tds_write_addtid_b32 %1 offset:%5\n\tds_write_addtid_b32 %2 offset:%6"
                 :: "v"(a), "v"(b), "v"(c), "s"(m0v), "i"(OFF), "i"(OFF + TROW), "i"(OFF + 2 * TROW) : "memory");
}
DEV void lds_drain() { asm volatile("s_waitcnt lgkmcnt(0)" ::: "memory"); }
DEV v4u ld_sc1(__amdgpu_buffer_rsrc_t rs, int off) { return __builtin_amdgcn_raw_buffer_load_b128(rs, off, 0, 16); }
DEV void st_sc1(__amdgpu_buffer_rsrc_t rs, int off, v4u v) { __builtin_amdgcn_raw_buffer_store_b128(v, rs, off, 0, 16); }
DEV void drain() { asm volatile("s_waitcnt vmcnt(0)" ::: "memory"); }
DEV void vm_clean() { __builtin_amdgcn_s_waitcnt(0x0F70); }
DEV void cbar() { asm volatile("" ::: "memory"); }

constexpr u32 kidx(int lag) {
    if (lag < 0 || lag >= L) return 660u;
    const int sh = lag < 256 ? 0 : (lag < 512 ? 1 : (lag < 1024 ? 2 : 3));
    const int k = sh ? ((lag + (1 << (sh - 1)) - 1 + ((lag >> sh) & 1)) >> sh) : lag;
    return (u32)(128 * sh + k);
}
struct GatherTab { u32 v[66][8]; };
constexpr GatherTab make_gather_tab() {
    GatherTab t{};
    for (int i = 0; i < 66; ++i) {
        const int a = i < 65 ? 194 + i : 1;
        for (int k = 0; k < 6; ++k) t.v[i][k] = (kidx(Z0 - 8 * a - 2 * k) * 2) | (kidx(Z0 - 8 * a - 2 * k - 1) * 2 << 16);
    }
    return t;
}

template <int N0, int N1>
DEV void conv_load_frags(u32 kc, LAS const unsigned char* sm, int lane, h8 (&A)[29]) {
    const int q = lane & 15, g = lane >> 4;
    const int s0 = Z0 - q + 8 * g;
    const int sg = s0 & 3;
    u32 pl = kc + sg * KCS + 2 * (s0 - sg) - 64 * (N1 - 1);
    asm volatile("" : "+v"(pl));
#pragma unroll
    for (int n = N0; n < N1; ++n) {
        const v2u lo = *(LAS const v2u*)(sm + pl + 64 * (N1 - 1 - n));
        asm volatile("");
        const v2u hi = *(LAS const v2u*)(sm + pl + 64 * (N1 - 1 - n) + 8);
        asm volatile("");
        const v4u t = {lo.x, lo.y, hi.x, hi.y};
        A[n - N0] = __builtin_bit_cast(h8, t);
    }
}
template <int N0, int N1, int DEPTH>
DEV void conv_run(const h8 (&A)[29], LAS const unsigned char* ub_lo, f4 (&acc)[8]) {
    constexpr int NU_HI = N1 - 1 < 8 ? N1 - 1 : 8;
    constexpr int NU_LO = N0 - 56;
    constexpr int STEPS = NU_HI - NU_LO + 1;
    h8 B[DEPTH];
    constexpr int T0 = N0 <= 8 ? 0 : (N0 - 8 + 7) / 8;
#pragma unroll
    for (int T = T0; T < 8; ++T) asm volatile("" : "+v"(acc[T]));
    asm volatile("s_nop 4");
#pragma unroll
    for (int k = 0; k < DEPTH; ++k) B[k] = *(LAS const h8*)(ub_lo + 64 * (8 - (NU_HI - k)));
    __builtin_amdgcn_sched_barrier(0);
#pragma unroll
    for (int k = 0; k < STEPS; ++k) {
        const int nu = NU_HI - k;
#pragma unroll
        for (int T = 0; T < 8; ++T) {
            const int n = nu + 8 * T;
            if (n >= N0 && n < N1) asm volatile("v_mfma_f32_16x16x32_f16 %0, %1, %2, %0" : "+v"(acc[T]) : "v"(A[n - N0]), "v"(B[k % DEPTH]));
        }
        if (k + DEPTH < STEPS) B[k % DEPTH] = *(LAS const h8*)(ub_lo + 64 * (8 - (nu - DEPTH)));
        __builtin_amdgcn_sched_barrier(0);
    }
    asm volatile("s_nop 15\n\ts_nop 7");
#pragma unroll
    for (int T = T0; T < 8; ++T) asm volatile("" : "+v"(acc[T]));
}
DEV constexpr int pslot(int job, int T) { return job == 0 ? T : (job == 1 ? 7 + T : 11 + T); }

#define STAMP(i) do { if (tid == STAMP_TID) { *(volatile LAS u64*)(sm + M_STAMP + 8 * (i)) = __builtin_amdgcn_s_memrealtime(); } } while (0)
#define PSTAMP(i) do {} while (0)
#define LSTAMP(i) STAMP(i)
#define STAMP_TID 0
}

__device__ const GatherTab gather_tab = make_gather_tab();

__global__ void __launch_bounds__(NT, 3)
s4d_moe_fused(const u16* __restrict__ x, const u16* __restrict__ pLogA, const u16* __restrict__ pAim,
              const u16* __restrict__ pCr, const u16* __restrict__ pCi, const u16* __restrict__ pLogDt,
              const u16* __restrict__ pDs, const u16* __restrict__ pWr, const u16* __restrict__ pWg,
              const u16* __restrict__ pWu, const u16* __restrict__ pWd, u16* __restrict__ out,
              unsigned char* __restrict__ ws) {
    __shared__ __attribute__((aligned(16))) unsigned char smem_[160 * 1024];
    LAS unsigned char* sm = (LAS unsigned char*)smem_;
    asm volatile("" :: "s"(x), "s"(pLogA), "s"(pAim), "s"(pCr), "s"(pCi), "s"(pLogDt), "s"(pDs), "s"(pWr), "s"(pWg), "s"(pWu), "s"(pWd),
                 "s"(out), "s"(ws));
    u64 pc0;
    asm volatile("s_getpc_b64 %0" : "=s"(pc0));
    const int tid = threadIdx.x, lane = tid & 63;
    const int w = __builtin_amdgcn_readfirstlane(tid >> 6);
    const int b = blockIdx.x, grp = b & 7, slot = b >> 3;
    const bool is_conv = slot < 27;
    const int d0 = 80 * grp + 3 * slot;
    const int nch = is_conv ? (slot == 26 ? 2 : 3) : 0;
    const int f = (slot - 27) * 8 + grp;
    const __amdgpu_buffer_rsrc_t rws = __builtin_amdgcn_make_buffer_rsrc((void*)ws, 0, WS_END, 0x00020000);

    STAMP(0);
    STAMP(25);
    if (tid == STAMP_TID) *(volatile LAS u64*)(sm + M_STAMP + 8 * 23) = __builtin_amdgcn_s_memtime();
    if (tid == 0) { *(LAS u32*)(sm + M_CLR) = 0; *(LAS u32*)(sm + M_CCNT) = 0; *(LAS u32*)(sm + M_P1) = 0; *(LAS u32*)(sm + M_P3) = 0; }

    u32 xrow[5], wr[5];
    if (w < 8) {
        const u32* xr = (const u32*)(x + (size_t)(8 * b + w) * D);
#pragma unroll
        for (int i = 0; i < 5; ++i) xrow[i] = xr[lane + 64 * i];
    }
    __syncthreads();
    u32 epoch = __builtin_amdgcn_raw_buffer_load_b32(rws, WS_EPOCH, 0, 0);
    auto get_tag = [&]() { u32 t = epoch; asm volatile("" : "+v"(t)); return t + 1u; };

    u32 tch[CODE_KB / 8 + 8];
#pragma unroll
    for (int i = 0; i < CODE_KB / 8 + 8; ++i) tch[i] = 0;
    auto touch_all = [&]() {
        typedef const __attribute__((address_space(1))) unsigned char* gptr;
        typedef const __attribute__((address_space(1))) u32* gptr32;
        const gptr pc = (gptr)(pc0 & ~127ull);
#pragma unroll
        for (int i = 0; i < CODE_KB / 8; ++i) tch[i] = *(gptr32)(pc + (i * 64 + lane) * 128);
#pragma unroll
        for (int i = 0; i < 3; ++i) tch[CODE_KB / 8 + i] = __builtin_amdgcn_raw_buffer_load_b32(rws, (i * 64 + lane) * 65536, 0, 0);
        tch[CODE_KB / 8 + 3] = *(const u32*)((const unsigned char*)out + min(lane * 65536, L * D * 2 - 4));
        tch[CODE_KB / 8 + 4] = *(const u32*)((const unsigned char*)pWg + min(lane * 32768, D * F * 2 - 4));
        tch[CODE_KB / 8 + 5] = *(const u32*)((const unsigned char*)pWu + min(lane * 32768, D * F * 2 - 4));
        tch[CODE_KB / 8 + 6] = *(const u32*)((const unsigned char*)pWd + min(lane * 32768, D * F * 2 - 4));
        tch[CODE_KB / 8 + 7] = *(const u32*)((const unsigned char*)pWr + (lane & 7) * 128);
    };
    auto touch_done = [&]() {
#pragma unroll
        for (int i = 0; i < CODE_KB / 8 + 8; ++i) asm volatile("" :: "v"(tch[i]));
    };

    auto p1_rows = [&]() {
        float m, s, rs;
        __builtin_amdgcn_s_setprio(3);
        ln_stats(xrow, m, s, rs);
#pragma unroll
        for (int i = 0; i < 5; ++i) {
            const u32 ub = ln_apply2(bf_lo(xrow[i]), bf_hi(xrow[i]), m, s, rs);
            *(LAS u32*)(sm + LD_UT + w * HT_ROW + (lane + 64 * i) * 4) = pk_h(bf_lo(ub), bf_hi(ub));
        }
        lds_drain();
        if (lane == 0) __hip_atomic_fetch_add((LAS u32*)(sm + M_P1), 1u, __ATOMIC_RELAXED, __HIP_MEMORY_SCOPE_WORKGROUP);
        __builtin_amdgcn_s_setprio(0);
        STAMP(1);
    };
    auto p1_store = [&]() {
        {
            {
                u32 sp = 0;
                while (__hip_atomic_load((LAS u32*)(sm + M_P1), __ATOMIC_RELAXED, __HIP_MEMORY_SCOPE_WORKGROUP) < 8u && ++sp < (1u << 22))
                    __builtin_amdgcn_s_sleep(1);
                cbar();
                PSTAMP(16);
            }
            auto tr = [&](int grp64) {
                const int cp = 64 * grp64 + (lane >> 1) + 32 * (lane & 1);
                u32 v[8];
#pragma unroll
                for (int k = 0; k < 8; ++k) v[k] = *(LAS const u32*)(sm + LD_UT + k * HT_ROW + cp * 4);
                const bool odd = lane & 1;
                u32 keep[4], recv[4];
#pragma unroll
                for (int k = 0; k < 4; ++k) {
                    const u32 lo = __builtin_amdgcn_perm(v[2 * k + 1], v[2 * k], 0x05040100u), hi = __builtin_amdgcn_perm(v[2 * k + 1], v[2 * k], 0x07060302u);
                    keep[k] = odd ? hi : lo;
                    recv[k] = dppu<0xB1>(odd ? lo : hi);
                }
                const v4u a = {odd ? recv[0] : keep[0], odd ? recv[1] : keep[1], odd ? recv[2] : keep[2], odd ? recv[3] : keep[3]};
                const v4u c = {odd ? keep[0] : recv[0], odd ? keep[1] : recv[1], odd ? keep[2] : recv[2], odd ? keep[3] : recv[3]};
                st_sc1(rws, WS_UX + b * (D * 16) + (128 * grp64 + lane) * 16, a);
                st_sc1(rws, WS_UX + b * (D * 16) + (128 * grp64 + 64 + lane) * 16, c);
            };
            tr(w - 9);
            if (w < 11) tr(w - 6);
            PSTAMP(18);
        }
    };
    auto p1_flag = [&]() {
        {
            drain();
            PSTAMP(19);
            u32 old = 0;
            if (lane == 0) old = __hip_atomic_fetch_add((LAS u32*)(sm + M_CLR), 1u, __ATOMIC_RELAXED, __HIP_MEMORY_SCOPE_WORKGROUP);
            old = __builtin_amdgcn_readfirstlane(old);
            if (old == 2u && lane < 8) __builtin_amdgcn_raw_buffer_store_b32(get_tag(), rws, WS_G1 + lane * 1024 + b * 4, 0, 16);
        }
    };
    auto clear_slots = [&]() {
        const v4u sv = {SENT, SENT, SENT, SENT};
        const int t = tid - 576;
#pragma unroll
        for (int i = 0; i < 4; ++i) st_sc1(rws, WS_HX + b * HXB + (t + 192 * i) * 16, sv);
        if (t < 96) st_sc1(rws, WS_HX + b * HXB + (768 + t) * 16, sv);
        if (w == 10) {
            if (lane >= 32 && lane < 36) st_sc1(rws, WS_G3 + b * 64 + (lane - 32) * 16, sv);
            if (!is_conv && f < 8 && lane >= 36 && lane < 38) st_sc1(rws, WS_IDX + f * 256 + (lane - 36) * 16, sv);
        }
        if (w == 11 && !is_conv && f < 24) st_sc1(rws, WS_ACT + (lane >> 3) * (F * 2) + f * 128 + (lane & 7) * 16, sv);
    };
    auto clear_rows = [&]() {
        const v4u sv = {SENT, SENT, SENT, SENT};
        const int t = tid - 576;
#pragma unroll
        for (int i = 0; i < 3; ++i) st_sc1(rws, WS_HROW + b * (D * 16) + (t + 192 * i) * 16, sv);
        if (w == 9) st_sc1(rws, WS_HROW + b * (D * 16) + (576 + lane) * 16, sv);
        drain();
    };
    if (w >= 9) clear_slots();

    v4u wf[20];
    if (is_conv) {
        u16 pla[3] = {0, 0, 0}, pai[3] = {0, 0, 0}, pcr[3] = {0, 0, 0}, pci[3] = {0, 0, 0};
        const int ad = min(d0 + (w < 9 ? w / 3 : w - 9), D - 1);
        const u16 pdt = pLogDt[ad];
        if (w < 9) {
            const int n = (w % 3) * 64 + lane;
            pla[0] = pLogA[ad * NS + n];
            pai[0] = pAim[ad * NS + n];
        } else {
#pragma unroll
            for (int i = 0; i < 3; ++i) {
                pla[i] = pLogA[ad * NS + i * 64 + lane];
                pai[i] = pAim[ad * NS + i * 64 + lane];
                pcr[i] = pCr[ad * NS + i * 64 + lane];
                pci[i] = pCi[ad * NS + i * 64 + lane];
            }
        }
        const int eo = tid / 3, ec = tid - 3 * eo;
        const u16 pdsk = pDs[min(d0 + min(tid & 3, 2), D - 1)];
        u32 gi[6], gi2[6];
        {
            const v4u* gt = (const v4u*)gather_tab.v[lane];
            const v4u g0 = gt[0], g1 = gt[1];
            gi[0] = g0.x; gi[1] = g0.y; gi[2] = g0.z; gi[3] = g0.w; gi[4] = g1.x; gi[5] = g1.y;
            const v4u* gu = (const v4u*)gather_tab.v[64 + (lane & 1)];
            const v4u g2 = gu[0], g3 = gu[1];
            gi2[0] = g2.x; gi2[1] = g2.y; gi2[2] = g2.z; gi2[3] = g2.w; gi2[4] = g3.x; gi2[5] = g3.y;
        }
        if (w == 8) touch_all();
        if (w < 8) p1_rows();
        if (w < 9) {
            const int c = w / 3;
            const u32 mb = LD_TBL + c * TCH + (w % 3) * 256;
            const float dt = rbf(__expf(bf2f(pdt)));
            const float zr = -rbf(__expf(bf2f(pla[0]))) * dt;
            const float zi = -bf2f(pai[0]) * dt;
            const cf B1 = cexp_pos(zr, zi, 1.f), H1 = cexp_pos(zr, zi, 128.f);
            const cf B2 = csq(B1), B3 = cmul(B2, B1), B4 = csq(B2), B5 = cmul(B4, B1), B6 = csq(B3), B7 = cmul(B4, B3);
            u32 hb[8];
            hb[0] = 0x00003C00u;
            hb[1] = c2h(B1); hb[2] = c2h(B2); hb[3] = c2h(B3); hb[4] = c2h(B4); hb[5] = c2h(B5); hb[6] = c2h(B6); hb[7] = c2h(B7);
            wr8<16 * TROW>(mb, hb[0], hb[1], hb[2], hb[3], hb[4], hb[5], hb[6], hb[7]);
            const cf H2 = csq(H1), H3 = cmul(H2, H1), H4 = csq(H2), H5 = cmul(H4, H1), H6 = csq(H3), H7 = cmul(H4, H3), H8 = csq(H4);
            const u32 h1 = c2h(H1), h2_ = c2h(H2), h3 = c2h(H3), h4 = c2h(H4), h5 = c2h(H5), h6 = c2h(H6), h7 = c2h(H7);
            wr3<24 * TROW>(mb, h1, hcmul(h1, hb[1]), hcmul(h1, hb[2]));
            const u32 mh = mb + 26 * TROW;
            wr8<1 * TROW>(mh, hcmul(h1, hb[3]), hcmul(h1, hb[4]), hcmul(h1, hb[5]), hcmul(h1, hb[6]), hcmul(h1, hb[7]),
                          h2_, hcmul(h2_, hb[2]), hcmul(h2_, hb[4]));
            wr8<9 * TROW>(mh, hcmul(h2_, hb[6]), h3, hcmul(h3, hb[2]), hcmul(h3, hb[4]), hcmul(h3, hb[6]),
                          h4, hcmul(h4, hb[4]), h5);
            wr8<17 * TROW>(mh, hcmul(h5, hb[4]), h6, hcmul(h6, hb[4]), h7, hcmul(h7, hb[4]), c2h(H8), c2h(cmul(H8, H1)),
                           c2h(csq(H5)));
            wr3<25 * TROW>(mh, c2h(cmul(H8, H3)), c2h(csq(H6)), c2h(cmul(H8, H5)));
            LAS u32* T = (LAS u32*)(sm + mb + lane * 4);
            T[54 * (TROW / 4)] = c2h(csq(H7));
            T[55 * (TROW / 4)] = c2h(cmul(H8, H7));
            T[56 * (TROW / 4)] = c2h(csq(H8));
            lds_drain();
        } else {
            __builtin_amdgcn_s_setprio(3);
            p1_store();
            p1_flag();
            __builtin_amdgcn_sched_barrier(0);
            const int c = w - 9;
            const float dt = rbf(__expf(bf2f(pdt)));
            float sabs = 0.f;
#pragma unroll
            for (int i = 0; i < 3; ++i) sabs += __builtin_fabsf(bf2f(pcr[i]) * dt) + __builtin_fabsf(bf2f(pci[i]) * dt);
            int e = 14 - __builtin_amdgcn_frexp_expf(wave_sum(sabs));
            e = e > 100 ? 100 : (e < -100 ? -100 : e);
            if (lane == 0) *(LAS int*)(sm + M_SCALE + 4 * c) = e;
            const float sc = __builtin_ldexpf(dt, e + 1);
            cf be[3], w8[3];
#pragma unroll
            for (int i = 0; i < 3; ++i) {
                w8[i] = cexp_pos(-rbf(__expf(bf2f(pla[i]))) * dt, bf2f(pai[i]) * dt, 8.f);
                be[i] = f2{bf2f(pcr[i]) * sc, bf2f(pci[i]) * sc};
            }
#pragma unroll
            for (int h = 0; h < 2; ++h) {
                u32 v[3][8];
#pragma unroll
                for (int j = 0; j < 8; ++j)
#pragma unroll
                    for (int i = 0; i < 3; ++i) {
                        v[i][j] = c2h(be[i]);
                        if (h == 0 || j < 7) be[i] = cmul(be[i], w8[i]);
                    }
#pragma unroll
                for (int i = 0; i < 3; ++i) {
                    const u32 mb = LD_TBL + c * TCH + i * 256;
                    if (h == 0) wr8<0>(mb, v[i][0], v[i][1], v[i][2], v[i][3], v[i][4], v[i][5], v[i][6], v[i][7]);
                    else wr8<8 * TROW>(mb, v[i][0], v[i][1], v[i][2], v[i][3], v[i][4], v[i][5], v[i][6], v[i][7]);
                }
            }
            if (lane < 16) *(LAS u16*)(sm + LD_KD + c * (KD_LEN * 2) + (656 + lane) * 2) = 0;
            lds_drain();
            __builtin_amdgcn_s_setprio(0);
        }
        touch_done();
        STAMP(2);
        __syncthreads();
        STAMP(3);
        if (w == 11) {
            const u32 tag = get_tag();
            __builtin_amdgcn_s_sleep(E1_DELAY);
            u32 sp = 0;
            while (true) {
                cbar();
                const v4u fl = ld_sc1(rws, WS_G1 + (b & 7) * 1024 + lane * 16);
                if (__all((fl.x == tag) & (fl.y == tag) & (fl.z == tag) & (fl.w == tag)) || ++sp > SPIN_CAP) break;
                __builtin_amdgcn_s_sleep(2);
            }
        }
        if (w < 9) {
            const int c = w / 3, tile = w % 3;
            const int j = lane & 15, g = lane >> 4;
            int row = 16 + 16 * tile + j;
            if (row > 56) row = 56;
            u32 pa = LD_TBL + c * TCH + row * TROW + g * 16, pb = LD_TBL + c * TCH + j * TROW + g * 16;
            asm volatile("" : "+v"(pa), "+v"(pb));
            h8 fa[12], fb[12];
#pragma unroll
            for (int t = 0; t < 12; ++t) {
                fa[t] = *(LAS const h8*)(sm + pa + 64 * t);
                fb[t] = *(LAS const h8*)(sm + pb + 64 * t);
            }
            __builtin_amdgcn_sched_barrier(0);
            f4 acc0 = {0.f, 0.f, 0.f, 0.f}, acc1 = {0.f, 0.f, 0.f, 0.f};
#pragma unroll
            for (int t = 0; t < 12; t += 2) {
                acc0 = __builtin_amdgcn_mfma_f32_16x16x32_f16(fa[t], fb[t], acc0, 0, 0, 0);
                acc1 = __builtin_amdgcn_mfma_f32_16x16x32_f16(fa[t + 1], fb[t + 1], acc1, 0, 0, 0);
            }
            const f4 acc = acc0 + acc1;
            const u32 p01 = pk_h(acc[0], acc[1]), p23 = pk_h(acc[2], acc[3]);
            LAS unsigned char* kd = sm + LD_KD + c * (KD_LEN * 2);
            if (tile == 0) {
                *(LAS v2u*)(kd + 2 * (128 * (g >> 1) + 4 * (g & 1) + 8 * j)) = v2u{p01, p23};
            } else if (tile == 1) {
                if (g < 2) {
                    *(LAS v2u*)(kd + 2 * (256 + 64 * g + 4 * j)) = v2u{p01, p23};
                } else {
                    *(LAS u32*)(kd + 2 * (384 + 64 * (g - 2) + 2 * j)) = p01;
                    *(LAS u32*)(kd + 2 * (384 + 64 * (g - 2) + 32 + 2 * j)) = p23;
                }
            } else {
                const int r0 = 4 * g;
                if (r0 <= 8) *(LAS u16*)(kd + 2 * (512 + 16 * r0 + j)) = (u16)p01;
                if (r0 + 3 <= 8) {
                    *(LAS u16*)(kd + 2 * (512 + 16 * (r0 + 1) + j)) = (u16)(p01 >> 16);
                    *(LAS u16*)(kd + 2 * (512 + 16 * (r0 + 2) + j)) = (u16)p23;
                    *(LAS u16*)(kd + 2 * (512 + 16 * (r0 + 3) + j)) = (u16)(p23 >> 16);
                }
            }
        }
        __syncthreads();
        cbar();
        const v4u uv = ld_sc1(rws, WS_UX + eo * (D * 16) + min(d0 + ec, D - 1) * 16);
        STAMP(4);
        {
            auto gather = [&](int c, const u32 (&ix)[6], u32 (&P)[6]) {
                LAS const unsigned char* kd = sm + LD_KD + c * (KD_LEN * 2);
#pragma unroll
                for (int k = 0; k < 6; ++k)
                    P[k] = (u32) * (LAS const u16*)(kd + (ix[k] & 0xffffu)) | ((u32) * (LAS const u16*)(kd + (ix[k] >> 16)) << 16);
            };
            auto put = [&](int c, int a, const u32 (&P)[6]) {
                LAS unsigned char* dst = sm + LD_KC + c * (4 * KCS) + a * 16;
                *(LAS v4u*)(dst) = v4u{P[0], P[1], P[2], P[3]};
                *(LAS v4u*)(dst + KCS) = v4u{__builtin_amdgcn_alignbit(P[1], P[0], 16), __builtin_amdgcn_alignbit(P[2], P[1], 16),
                                             __builtin_amdgcn_alignbit(P[3], P[2], 16), __builtin_amdgcn_alignbit(P[4], P[3], 16)};
                *(LAS v4u*)(dst + 2 * KCS) = v4u{P[1], P[2], P[3], P[4]};
                *(LAS v4u*)(dst + 3 * KCS) = v4u{__builtin_amdgcn_alignbit(P[2], P[1], 16), __builtin_amdgcn_alignbit(P[3], P[2], 16),
                                                 __builtin_amdgcn_alignbit(P[4], P[3], 16), __builtin_amdgcn_alignbit(P[5], P[4], 16)};
            };
            if (w < 8) {
#pragma unroll
                for (int i = 0; i < 5; ++i) *(LAS u32*)(sm + LD_XR + w * 1280 + (lane + 64 * i) * 4) = xrow[i];
            }
            const int cc = tid >> 8, ca = 2 + (tid & 255), kind = w & 3;
            LAS const unsigned char* kd = sm + LD_KD + cc * (KD_LEN * 2);
            u32 P[6], Q[6];
            if (kind < 2) {
                const int c0 = 642 - ca;
                const u32 v0 = *(LAS const u16*)(kd + 2 * c0), v1 = *(LAS const u16*)(kd + 2 * c0 - 2);
                const u32 w00 = v0 | (v0 << 16), w11 = v1 | (v1 << 16);
                P[0] = ca == 2 ? (v0 << 16) : w00;
                P[1] = w00;
                P[2] = (ca & 1) ? w11 : (v0 | (v1 << 16));
                P[3] = w11; P[4] = w11; P[5] = w11;
            } else if (kind == 2) {
                const int c0 = 772 - 2 * ca;
                const u32 hi = *(LAS const u32*)(kd + 2 * c0), lo = *(LAS const u32*)(kd + 2 * c0 - 4);
                P[0] = __builtin_amdgcn_perm(hi, hi, 0x01000100u);
                P[1] = __builtin_amdgcn_perm(hi, lo, 0x03020504u);
                P[2] = __builtin_amdgcn_perm(lo, lo, 0x03020302u);
                P[3] = __builtin_amdgcn_perm(lo, lo, 0x01000100u);
                P[4] = P[3]; P[5] = P[3];
            } else {
                gather(cc, gi, P);
            }
            if (w == 11) gather((lane >> 1) % 3, gi2, Q);
            put(cc, ca, P);
            if (w == 11) {
                if (lane < 6) put(lane >> 1, (lane & 1) ? 1 : 258, Q);
            } else if (w == 10) {
                for (int i = lane; i < 72; i += 64) {
                    const int cs = i / 6, k = i % 6;
                    *(LAS v4u*)(sm + LD_KC + (cs >> 2) * (4 * KCS) + (cs & 3) * KCS + (k == 0 ? 0 : 258 + k) * 16) = v4u{0, 0, 0, 0};
                }
            } else if (w == 9) {
                for (int i = lane; i < 102; i += 64) {
                    const int c = i / 34, k = i % 34;
                    const int el = k < 32 ? 8 * k : 2304 + 8 * (k - 32);
                    *(LAS v4u*)(sm + LD_UU + c * (ULEN * 2) + el * 2) = v4u{0, 0, 0, 0};
                }
            }
        }
        __syncthreads();
        STAMP(5);
        int job = -1, jc = 0;
        if ((w & 3) == 0) { job = 2; jc = w >> 2; }
        else if (w < 8) { job = (w >> 2); jc = (w & 3) - 1; }
        h8 A[29];
        const u32 kc = LD_KC + jc * (4 * KCS);
        if (job == 0) conv_load_frags<0, 16>(kc, sm, lane, A);
        else if (job == 1) conv_load_frags<16, 36>(kc, sm, lane, A);
        else if (job == 2) conv_load_frags<36, 65>(kc, sm, lane, A);
        STAMP(6);
        *(LAS v4u*)(sm + LD_UU + ec * (ULEN * 2) + (256 + 8 * eo) * 2) = uv;
        STAMP(7);
        __syncthreads();
        STAMP(8);
        if (job >= 0) {
            f4 acc[8];
#pragma unroll
            for (int T = 0; T < 8; ++T) acc[T] = f4{0.f, 0.f, 0.f, 0.f};
            const int p = lane & 15, g = lane >> 4;
            u32 uba = LD_UU + jc * (ULEN * 2) + (16 * p + 8 * g) * 2;
            asm volatile("" : "+v"(uba));
            LAS const unsigned char* ub = sm + uba;
            LAS unsigned char* part = sm + LD_PART + jc * PT_CH + g * PT_G + p * 16;
            if (job == 0) {
                conv_run<0, 16, DEP_L>(A, ub, acc);
#pragma unroll
                for (int T = 0; T < 8; ++T) *(LAS f4*)(part + pslot(0, T) * PT) = acc[T];
            } else if (job == 1) {
                conv_run<16, 36, DEP_M>(A, ub, acc);
#pragma unroll
                for (int T = 1; T < 8; ++T) *(LAS f4*)(part + pslot(1, T) * PT) = acc[T];
            } else {
                conv_run<36, 65, DEP_H>(A, ub, acc);
#pragma unroll
                for (int T = 4; T < 8; ++T) *(LAS f4*)(part + pslot(2, T) * PT) = acc[T];
            }
        } else {
            clear_rows();
        }
        STAMP(9);
        __syncthreads();
        STAMP(10);
        {
            const float dsk = bf2f(pdsk);
            const int pc = tid & 3, cc = pc < 3 ? pc : 2;
            const int e = *(LAS const int*)(sm + M_SCALE + 4 * cc);
            const float isc = __builtin_ldexpf(1.f, -e);
            auto piece = [&](int eo_, float (&y)[8]) {
                const int T = eo_ >> 5, p = (eo_ >> 1) & 15, g0 = (eo_ & 1) * 2;
                LAS const unsigned char* part = sm + LD_PART + cc * PT_CH + g0 * PT_G + p * 16;
                const int sM = T >= 1 ? pslot(1, T) : 0, sH = T >= 4 ? pslot(2, T) : 0;
                const float kM = T >= 1 ? 1.f : 0.f, kH = T >= 4 ? 1.f : 0.f;
                const f4 l0 = *(LAS const f4*)(part + T * PT), l1 = *(LAS const f4*)(part + T * PT + PT_G);
                const f4 m0 = *(LAS const f4*)(part + sM * PT), m1 = *(LAS const f4*)(part + sM * PT + PT_G);
                const f4 h0 = *(LAS const f4*)(part + sH * PT), h1 = *(LAS const f4*)(part + sH * PT + PT_G);
                const v4u ur = *(LAS const v4u*)(sm + LD_UU + cc * (ULEN * 2) + (256 + 8 * eo_) * 2);
                const f4 y0 = (l0 + m0 * kM + h0 * kH) * isc, y1 = (l1 + m1 * kM + h1 * kH) * isc;
#pragma unroll
                for (int r = 0; r < 4; ++r) { y[r] = y0[r]; y[4 + r] = y1[r]; }
                const u32 uu[4] = {ur.x, ur.y, ur.z, ur.w};
                u32 o[4];
#pragma unroll
                for (int k = 0; k < 4; ++k) {
                    const h2 u2 = as_h2(uu[k]);
                    const u32 yb = pk_bf(y[2 * k], y[2 * k + 1]), ud = pk_bf((float)u2.x * dsk, (float)u2.y * dsk);
                    o[k] = pk_bf(bf_lo(yb) + bf_lo(ud), bf_hi(yb) + bf_hi(ud));
                }
                const bool real = pc < nch;
                return v4u{real ? o[0] : 0u, real ? o[1] : 0u, real ? o[2] : 0u, real ? o[3] : 0u};
            };
            const int pid = grp * 27 + slot;
            float ya[8], yb_[8];
            const int o1 = tid >> 2, o2 = 192 + ((tid - 512) >> 2);
            const v4u va = piece(o1, ya);
            v4u vb;
            if (w >= 8) vb = piece(o2, yb_);
            __builtin_amdgcn_raw_buffer_store_b128(va, rws, WS_HX + o1 * HXB + pid * 64 + pc * 16, 0, HX_AUX);
            if (w >= 8) __builtin_amdgcn_raw_buffer_store_b128(vb, rws, WS_HX + o2 * HXB + pid * 64 + pc * 16, 0, HX_AUX);
            STAMP(11);
            drain();
        }
    } else {
        v4u rw[16];
        auto load_weights = [&]() {
            if (f < 24) {
#pragma unroll
                for (int i = 0; i < 7; ++i) {
                    const int p = min(tid + NT * i, 5119);
                    rw[i] = *(const v4u*)(pWg + (size_t)(p >> 3) * F + 64 * f + 8 * (p & 7));
                    rw[8 + i] = *(const v4u*)(pWu + (size_t)(p >> 3) * F + 64 * f + 8 * (p & 7));
                }
            } else if (f < 34) {
#pragma unroll
                for (int i = 0; i < 16; ++i) {
                    const int p = tid + NT * i;
                    rw[i] = *(const v4u*)(pWd + (size_t)(p >> 3) * D + 64 * (f - 24) + 8 * (p & 7));
                }
            }
        };
        if (w == 8) touch_all();
        if (w < 8) p1_rows();
        if (w >= 9) { __builtin_amdgcn_s_setprio(3); p1_store(); p1_flag(); __builtin_amdgcn_s_setprio(0); }
        touch_done();
        load_weights();
        const int g = lane >> 4, q = (lane & 15) >> 2, pp = lane & 3;
        if (f < 24) {
#pragma unroll
            for (int rnd = 0; rnd < 2; ++rnd) {
#pragma unroll
                for (int i = 0; i < 7; ++i) {
                    const int p = tid + NT * i;
                    if (p < 5120) *(LAS v4u*)(sm + LF_W + p * 16) = rw[8 * rnd + i];
                }
                __syncthreads();
                if (w < 8 && (w >> 2) == rnd) {
                    LAS const unsigned char* base = sm + LF_W + (w & 3) * 32 + pp * 8;
#pragma unroll
                    for (int s = 0; s < 20; ++s) {
                        const int k0 = 32 * s + 8 * g + q;
                        const s4 a = __builtin_amdgcn_ds_read_tr16_b64_v4i16((LAS s4*)(base + k0 * 128));
                        const s4 c = __builtin_amdgcn_ds_read_tr16_b64_v4i16((LAS s4*)(base + (k0 + 4) * 128));
                        const v2u a2 = __builtin_bit_cast(v2u, a), c2 = __builtin_bit_cast(v2u, c);
                        wf[s] = v4u{a2.x, a2.y, c2.x, c2.y};
                    }
                }
                __syncthreads();
            }
        } else if (f < 34) {
#pragma unroll
            for (int rnd = 0; rnd < 2; ++rnd) {
#pragma unroll
                for (int i = 0; i < 8; ++i) *(LAS v4u*)(sm + LF_W + (tid + NT * i) * 16) = rw[8 * rnd + i];
                __syncthreads();
                LAS const unsigned char* base = sm + LF_W + (w & 3) * 32 + pp * 8;
#pragma unroll
                for (int s = 0; s < 16; ++s) {
                    const int ks = 16 * (w >> 2) + s;
                    if ((ks >= 24) == (rnd == 1)) {
                        const int k0 = 32 * (ks - 24 * rnd) + 8 * g + q;
                        const s4 a = __builtin_amdgcn_ds_read_tr16_b64_v4i16((LAS s4*)(base + k0 * 128));
                        const s4 c = __builtin_amdgcn_ds_read_tr16_b64_v4i16((LAS s4*)(base + (k0 + 4) * 128));
                        const v2u a2 = __builtin_bit_cast(v2u, a), c2 = __builtin_bit_cast(v2u, c);
                        wf[s] = v4u{a2.x, a2.y, c2.x, c2.y};
                    }
                }
                __syncthreads();
            }
        }
        if (w >= 9) clear_rows();
        if (w < 8) {
#pragma unroll
            for (int i = 0; i < 5; ++i) *(LAS u32*)(sm + LD_XR + w * 1280 + (lane + 64 * i) * 4) = xrow[i];
        }
        __syncthreads();
        if (w == 0) {
            u32 sp = 0;
            while (true) {
                cbar();
                const u32 v = __builtin_amdgcn_raw_buffer_load_b32(rws, WS_HX + b * HXB, 0, 16);
                if (v != SENT || ++sp > SPIN_CAP) break;
                __builtin_amdgcn_s_sleep(FFN_IDLE);
            }
        }
    }
    if (w < 8) {
#pragma unroll
        for (int i = 0; i < 5; ++i) wr[i] = ((const u32*)pWr)[lane + 64 * i];
    }
    vm_clean();
    __syncthreads();
    STAMP(12);

    if (tid < D) {
        v4u v;
        u32 sp = 0;
        const int cg = tid / 80, cr = tid - 80 * cg, cs = cr / 3;
        const int hxo = WS_HX + b * HXB + (cg * 27 + cs) * 64 + (cr - 3 * cs) * 16;
        if (P3_DELAY && is_conv) __builtin_amdgcn_s_sleep(P3_DELAY);
        while (true) {
            cbar();
            v = ld_sc1(rws, hxo);
            const bool ok = (v.x != SENT) & (v.y != SENT) & (v.z != SENT) & (v.w != SENT);
            if (__all(ok) || ++sp > SPIN_CAP) break;
            if (P3_BACK) __builtin_amdgcn_s_sleep(P3_BACK);
        }
        STAMP(13);
        const bool odd = tid & 1;
        const u32 s0 = odd ? v.x : v.z, s1 = odd ? v.y : v.w;
        const u32 r0 = dppu<0xB1>(s0), r1 = dppu<0xB1>(s1);
        const u32 k0 = odd ? v.z : v.x, k1 = odd ? v.w : v.y;
        const u32 lo0 = odd ? r0 : k0, hi0 = odd ? k0 : r0, lo1 = odd ? r1 : k1, hi1 = odd ? k1 : r1;
        LAS u32* dst = (LAS u32*)(sm + LD_HT + (odd ? 4 * HT_ROW : 0) + (tid >> 1) * 4);
        dst[0 * (HT_ROW / 4)] = __builtin_amdgcn_perm(hi0, lo0, 0x05040100u);
        dst[1 * (HT_ROW / 4)] = __builtin_amdgcn_perm(hi0, lo0, 0x07060302u);
        dst[2 * (HT_ROW / 4)] = __builtin_amdgcn_perm(hi1, lo1, 0x05040100u);
        dst[3 * (HT_ROW / 4)] = __builtin_amdgcn_perm(hi1, lo1, 0x07060302u);
    }
    __syncthreads();
    u32 orow[5];
    const int mytok = 8 * b + w;
    if (w < 8) {
        u32 hv[5];
#pragma unroll
        for (int i = 0; i < 5; ++i) {
            const u32 a = *(LAS const u32*)(sm + LD_HT + w * HT_ROW + (lane + 64 * i) * 4);
            const u32 xv = *(LAS const u32*)(sm + LD_XR + w * 1280 + (lane + 64 * i) * 4);
            hv[i] = pk_bf(bf_lo(xv) + bf_lo(a), bf_hi(xv) + bf_hi(a));
        }
        float m, s, rs;
        ln_stats(hv, m, s, rs);
        float racc = 0.f;
#pragma unroll
        for (int i = 0; i < 5; ++i) {
            const u32 z = ln_apply2(bf_lo(hv[i]), bf_hi(hv[i]), m, s, rs);
            racc = __builtin_fmaf(bf_lo(z), bf_lo(wr[i]), __builtin_fmaf(bf_hi(z), bf_hi(wr[i]), racc));
            orow[i] = pk_bf(bf_lo(hv[i]) + bf_lo(z), bf_hi(hv[i]) + bf_hi(z));
        }
        const float r = wave_sum(racc);
        u32 old = 0;
        if (lane == 0) {
            *(LAS v2u*)(sm + M_G3 + w * 8) = v2u{(__float_as_uint(m) >> 16) | (__float_as_uint(s) & 0xffff0000u), pk_bf(r, 0.f) & 0xffffu};
            lds_drain();
            old = __hip_atomic_fetch_add((LAS u32*)(sm + M_P3), 1u, __ATOMIC_RELAXED, __HIP_MEMORY_SCOPE_WORKGROUP);
        }
        old = __builtin_amdgcn_readfirstlane(old);
        if (old == 7u && lane < 4) {
            cbar();
            st_sc1(rws, WS_G3 + b * 64 + lane * 16, *(LAS const v4u*)(sm + M_G3 + lane * 16));
        }
        STAMP(14);
    } else {
        if (HROW_DELAY) __builtin_amdgcn_s_sleep(HROW_DELAY);
        const int t = tid - 512;
        v4u a[3], xv[3];
#pragma unroll
        for (int i = 0; i < 3; ++i) {
            const int p = min(t + 256 * i, 639), k = p / 80, ch = p - 80 * k;
            a[i] = *(LAS const v4u*)(sm + LD_HT + k * HT_ROW + ch * 16);
            xv[i] = *(LAS const v4u*)(sm + LD_XR + k * 1280 + ch * 16);
        }
#pragma unroll
        for (int i = 0; i < 3; ++i) {
            const v4u h = {pk_bf(bf_lo(xv[i].x) + bf_lo(a[i].x), bf_hi(xv[i].x) + bf_hi(a[i].x)),
                           pk_bf(bf_lo(xv[i].y) + bf_lo(a[i].y), bf_hi(xv[i].y) + bf_hi(a[i].y)),
                           pk_bf(bf_lo(xv[i].z) + bf_lo(a[i].z), bf_hi(xv[i].z) + bf_hi(a[i].z)),
                           pk_bf(bf_lo(xv[i].w) + bf_lo(a[i].w), bf_hi(xv[i].w) + bf_hi(a[i].w))};
            if (i < 2 || w < 10) st_sc1(rws, WS_HROW + b * (D * 16) + (t + 256 * i) * 16, h);
        }
    }
    STAMP(15);

    if (!is_conv && f < 34) {
        u32 key[4], msv[4];
        if (w < 8) {
            __builtin_amdgcn_s_sleep(G3_DELAY);
            v4u g0, g1;
            u32 sp = 0;
            while (true) {
                cbar();
                g0 = ld_sc1(rws, WS_G3 + tid * 32);
                g1 = ld_sc1(rws, WS_G3 + tid * 32 + 16);
                const bool ok = (g0.y != SENT) & (g0.w != SENT) & (g1.y != SENT) & (g1.w != SENT);
                if (__all(ok) || ++sp > SPIN_CAP) break;
                if (G3_BACK) __builtin_amdgcn_s_sleep(G3_BACK);
            }
            LSTAMP(16);
            const u32 rb[4] = {g0.y, g0.w, g1.y, g1.w};
            msv[0] = g0.x; msv[1] = g0.z; msv[2] = g1.x; msv[3] = g1.z;
#pragma unroll
            for (int j = 0; j < 4; ++j) {
                const u32 o = (rb[j] & 0x8000u) ? (~rb[j] & 0xffffu) : (rb[j] | 0x8000u);
                key[j] = (o << 11) | (u32)(2047 - (4 * tid + j));
            }
            const u32 mx = wave_max(umax(umax(key[0], key[1]), umax(key[2], key[3])));
            if (lane == 0) *(LAS u32*)(sm + M_WMAX + 4 * w) = mx;
        }
        __syncthreads();
        if (w < 8) {
            const v4u a = *(LAS const v4u*)(sm + M_WMAX), c = *(LAS const v4u*)(sm + M_WMAX + 16);
            const u32 thr = min(min(min(a.x, a.y), min(a.z, a.w)), min(min(c.x, c.y), min(c.z, c.w)));
            u64 bal[4];
            int tot = 0;
#pragma unroll
            for (int j = 0; j < 4; ++j) { bal[j] = __ballot(key[j] >= thr); tot += __builtin_popcountll(bal[j]); }
            u32 base = 0;
            if (lane == 0) base = __hip_atomic_fetch_add((LAS u32*)(sm + M_CCNT), (u32)tot, __ATOMIC_RELAXED, __HIP_MEMORY_SCOPE_WORKGROUP);
            base = __builtin_amdgcn_readfirstlane(base);
#pragma unroll
            for (int j = 0; j < 4; ++j) {
                const u32 pos = base + __builtin_amdgcn_mbcnt_hi((u32)(bal[j] >> 32), __builtin_amdgcn_mbcnt_lo((u32)bal[j], 0));
                if (key[j] >= thr) *(LAS v2u*)(sm + LF_CAND + pos * 8) = v2u{key[j], msv[j]};
                base += __builtin_popcountll(bal[j]);
            }
        }
        __syncthreads();
        {
            const u32 cnt = *(LAS const u32*)(sm + M_CCNT);
            for (u32 i0 = 64 * w; i0 < cnt; i0 += NT) {
                const u32 i = i0 + lane;
                const v2u me = *(LAS const v2u*)(sm + LF_CAND + min(i, cnt - 1) * 8);
                u32 rank = 0;
                for (u32 j0 = 0; j0 < cnt; j0 += 64) {
                    const u32 ot = (j0 + lane) < cnt ? *(LAS const u32*)(sm + LF_CAND + (j0 + lane) * 8) : 0u;
                    const u32 nj = min(64u, cnt - j0);
                    for (u32 j = 0; j < nj; j += 4) {
                        rank += (u32)__builtin_amdgcn_readlane(ot, j) > me.x;
                        rank += (u32)__builtin_amdgcn_readlane(ot, j + 1) > me.x;
                        rank += (u32)__builtin_amdgcn_readlane(ot, j + 2) > me.x;
                        rank += (u32)__builtin_amdgcn_readlane(ot, j + 3) > me.x;
                    }
                }
                if (i < cnt && rank < 8) {
                    const u32 rbits = me.x >> 11;
                    const u32 rb = (rbits & 0x8000u) ? (rbits & 0x7fffu) : (~rbits & 0xffffu);
                    *(LAS v4u*)(sm + M_SEL + rank * 16) = v4u{2047u - (me.x & 2047u), me.y, rb, 0u};
                }
            }
        }
        __syncthreads();
        LSTAMP(17);
        if (tid < D) {
            const int k = tid / 80, ch = tid % 80;
            const v4u sel = *(LAS const v4u*)(sm + M_SEL + k * 16);
            v4u hv;
            u32 sp = 0;
            while (true) {
                cbar();
                hv = ld_sc1(rws, WS_HROW + (int)(sel.x & (L - 1)) * (D * 2) + ch * 16);
                const bool ok = (hv.x != SENT) & (hv.y != SENT) & (hv.z != SENT) & (hv.w != SENT);
                if (__all(ok) || ++sp > SPIN_CAP) break;
            }
            const float m = bf_lo(sel.y), s = bf_hi(sel.y), rs = __builtin_amdgcn_rcpf(s);
            const u32 hh[4] = {hv.x, hv.y, hv.z, hv.w};
            u32 zz[4];
#pragma unroll
            for (int i = 0; i < 4; ++i) zz[i] = ln_apply2(bf_lo(hh[i]), bf_hi(hh[i]), m, s, rs);
            *(LAS v4u*)(sm + LF_Z + k * HT_ROW + ch * 16) = v4u{zz[0], zz[1], zz[2], zz[3]};
            *(LAS v4u*)(sm + LF_H + k * HT_ROW + ch * 16) = hv;
        } else if (w == 11 && f < 8 && lane < 2) {
            const v4u s0 = *(LAS const v4u*)(sm + M_SEL + lane * 64), s1 = *(LAS const v4u*)(sm + M_SEL + lane * 64 + 16),
                      s2 = *(LAS const v4u*)(sm + M_SEL + lane * 64 + 32), s3 = *(LAS const v4u*)(sm + M_SEL + lane * 64 + 48);
            st_sc1(rws, WS_IDX + f * 256 + lane * 16, v4u{s0.x, s1.x, s2.x, s3.x});
        }
        __syncthreads();
        LSTAMP(18);
        const int row = lane & 7, g = lane >> 4;
        if (f < 24) {
            f4 acc = {0.f, 0.f, 0.f, 0.f};
            if (w < 8) {
                f4 acc1 = {0.f, 0.f, 0.f, 0.f};
                LAS const unsigned char* zb = sm + LF_Z + row * HT_ROW + 16 * g;
                b8 za[10];
#pragma unroll
                for (int s = 0; s < 10; ++s) za[s] = *(LAS const b8*)(zb + 64 * s);
                __builtin_amdgcn_sched_barrier(0);
#pragma unroll
                for (int s = 0; s < 20; s += 2) {
                    acc = __builtin_amdgcn_mfma_f32_16x16x32_bf16(za[s % 10], __builtin_bit_cast(b8, wf[s]), acc, 0, 0, 0);
                    acc1 = __builtin_amdgcn_mfma_f32_16x16x32_bf16(za[(s + 1) % 10], __builtin_bit_cast(b8, wf[s + 1]), acc1, 0, 0, 0);
                    if (s < 10) {
                        za[s] = *(LAS const b8*)(zb + 64 * (s + 10));
                        za[s + 1] = *(LAS const b8*)(zb + 64 * (s + 11));
                    }
                    __builtin_amdgcn_sched_barrier(0);
                }
                acc += acc1;
                if (w >= 4) *(LAS f4*)(sm + LF_RED + (w - 4) * 1024 + lane * 16) = acc;
            }
            __syncthreads();
            if (w < 4) {
                const f4 up = *(LAS const f4*)(sm + LF_RED + w * 1024 + lane * 16);
                float a[4];
#pragma unroll
                for (int r = 0; r < 4; ++r) {
                    const float gb = rbf(acc[r]);
                    const float sg = rbf(gb * __builtin_amdgcn_rcpf(1.f + __expf(-gb)));
                    a[r] = sg * rbf(up[r]);
                }
                if (g < 2) {
#pragma unroll
                    for (int r = 0; r < 4; r += 2) {
                        const u32 pr = pk_bf(a[r], a[r + 1]);
                        *(LAS u16*)(sm + LF_ACT + (4 * g + r) * 128 + (16 * w + (lane & 15)) * 2) = (u16)pr;
                        *(LAS u16*)(sm + LF_ACT + (4 * g + r + 1) * 128 + (16 * w + (lane & 15)) * 2) = (u16)(pr >> 16);
                    }
                }
                lds_drain();
                u32 old = 0;
                if (lane == 0) old = __hip_atomic_fetch_add((LAS u32*)(sm + M_P1), 1u, __ATOMIC_RELAXED, __HIP_MEMORY_SCOPE_WORKGROUP);
                old = __builtin_amdgcn_readfirstlane(old);
                if (old == 8u + 3u) {
                    cbar();
                    st_sc1(rws, WS_ACT + (lane >> 3) * (F * 2) + f * 128 + (lane & 7) * 16, *(LAS const v4u*)(sm + LF_ACT + lane * 16));
                }
            }
            LSTAMP(19);
        } else {
            {
                v4u a0, a1;
                u32 sp = 0;
                __builtin_amdgcn_s_sleep(ACT_DELAY);
                while (true) {
                    cbar();
                    a0 = ld_sc1(rws, WS_ACT + tid * 16);
                    a1 = ld_sc1(rws, WS_ACT + (tid + NT) * 16);
                    const bool ok = (a0.x != SENT) & (a0.y != SENT) & (a0.z != SENT) & (a0.w != SENT) & (a1.x != SENT) &
                                    (a1.y != SENT) & (a1.z != SENT) & (a1.w != SENT);
                    if (__all(ok) || ++sp > SPIN_CAP) break;
                    if (ACT_BACK) __builtin_amdgcn_s_sleep(ACT_BACK);
                }
                *(LAS v4u*)(sm + LF_ACT + (tid / 192) * ACT_ROW + (tid % 192) * 16) = a0;
                *(LAS v4u*)(sm + LF_ACT + ((tid + NT) / 192) * ACT_ROW + ((tid + NT) % 192) * 16) = a1;
            }
            __syncthreads();
            LSTAMP(20);
            f4 acc = {0.f, 0.f, 0.f, 0.f}, acc1 = {0.f, 0.f, 0.f, 0.f};
            LAS const unsigned char* ab = sm + LF_ACT + row * ACT_ROW + 1024 * (w >> 2) + 16 * g;
            b8 aa[16];
#pragma unroll
            for (int s = 0; s < 16; ++s) aa[s] = *(LAS const b8*)(ab + 64 * s);
            __builtin_amdgcn_sched_barrier(0);
#pragma unroll
            for (int s = 0; s < 16; s += 2) {
                acc = __builtin_amdgcn_mfma_f32_16x16x32_bf16(aa[s], __builtin_bit_cast(b8, wf[s]), acc, 0, 0, 0);
                acc1 = __builtin_amdgcn_mfma_f32_16x16x32_bf16(aa[s + 1], __builtin_bit_cast(b8, wf[s + 1]), acc1, 0, 0, 0);
            }
            acc += acc1;
            *(LAS f4*)(sm + LF_RED + w * 1024 + lane * 16) = acc;
            __syncthreads();
            if (tid < 512) {
                const int tok = tid >> 6, col = tid & 63, tile = col >> 4;
                const int off = ((tok >> 2) * 16 + (col & 15)) * 16 + (tok & 3) * 4;
                const float y = *(LAS const float*)(sm + LF_RED + tile * 1024 + off) + *(LAS const float*)(sm + LF_RED + (4 + tile) * 1024 + off) +
                                *(LAS const float*)(sm + LF_RED + (8 + tile) * 1024 + off);
                const v4u sel = *(LAS const v4u*)(sm + M_SEL + tok * 16);
                const int d = 64 * (f - 24) + col;
                const float z = bf2f(*(LAS const u16*)(sm + LF_Z + tok * HT_ROW + d * 2));
                const float h = bf2f(*(LAS const u16*)(sm + LF_H + tok * HT_ROW + d * 2));
                const float r = bf2f((u16)sel.z);
                const float sg = rbf(__builtin_amdgcn_rcpf(1.f + __expf(-r)));
                const float upd = rbf(rbf(y) * sg);
                out[(size_t)(sel.x & (L - 1)) * D + d] = (u16)pk_bf(h + rbf(z + upd), 0.f);
            }
        }
    }

    STAMP(21);
    if (w < 8) {
        v4u i0, i1;
        u32 sp = 0;
        while (true) {
            cbar();
            i0 = ld_sc1(rws, WS_IDX + (b & 7) * 256);
            i1 = ld_sc1(rws, WS_IDX + (b & 7) * 256 + 16);
            const bool ok = (i0.x != SENT) & (i0.y != SENT) & (i0.z != SENT) & (i0.w != SENT) & (i1.x != SENT) & (i1.y != SENT) &
                            (i1.z != SENT) & (i1.w != SENT);
            if (__all(ok) || ++sp > SPIN_CAP) break;
            __builtin_amdgcn_s_sleep(16);
        }
        const u32 t = (u32)mytok;
        const bool routed = (i0.x == t) | (i0.y == t) | (i0.z == t) | (i0.w == t) | (i1.x == t) | (i1.y == t) | (i1.z == t) | (i1.w == t);
        if (!routed) {
            const __amdgpu_buffer_rsrc_t ro = __builtin_amdgcn_make_buffer_rsrc((void*)out, 0, L * D * 2, 0x00020000);
#pragma unroll
            for (int i = 0; i < 5; ++i) __builtin_amdgcn_raw_buffer_store_b32(orow[i], ro, mytok * (D * 2) + (lane + 64 * i) * 4, 0, OUT_AUX);
        }
        if (b == 0 && tid == 0) *(u32*)(ws + WS_EPOCH) = get_tag();
    }
    STAMP(22);
    if (tid == STAMP_TID) {
        *(volatile LAS u64*)(sm + M_STAMP + 8 * 24) = __builtin_amdgcn_s_memtime();
        u64* ds = (u64*)(ws + WS_DBG + (14 << 20)) + b * 32;
        for (int i = 0; i < 26; ++i) ds[i] = *(volatile LAS u64*)(sm + M_STAMP + 8 * i);
        if (b == 0 || b == 100 || b == 211 || b == 216 || b == 240 || b == 255) {
            u32 t[23];
            const u64 t0 = *(volatile LAS u64*)(sm + M_STAMP);
            for (int i = 0; i < 23; ++i) t[i] = (u32)(*(volatile LAS u64*)(sm + M_STAMP + 8 * i) - t0);
            printf("WG %d t0 %llu: %u %u %u %u %u %u %u %u %u %u %u | %u %u %u %u %u %u %u %u %u %u %u\n", b, t0 % 1000000, t[1], t[2], t[3], t[4], t[5], t[6], t[7],
                   t[8], t[9], t[10], t[11], t[12], t[13], t[14], t[15], t[16], t[17], t[18], t[19], t[20], t[21], t[22]);
        }
    }
}

extern "C" void kernel_launch(void* const* d_in, const int* in_sizes, int n_in, void* d_out, int out_size, void* d_ws,
                              size_t ws_size, hipStream_t stream) {
    (void)in_sizes; (void)n_in; (void)out_size; (void)ws_size;
    s4d_moe_fused<<<256, NT, 0, stream>>>((const u16*)d_in[0], (const u16*)d_in[1], (const u16*)d_in[2], (const u16*)d_in[3],
                                          (const u16*)d_in[4], (const u16*)d_in[5], (const u16*)d_in[6],
                                          (const u16*)d_in[8], (const u16*)d_in[9], (const u16*)d_in[10],
                                          (const u16*)d_in[11], (u16*)d_out, (unsigned char*)d_ws);
}
```

```cpp
#include <hip/hip_runtime.h>
#include <stdint.h>

typedef unsigned int u32;
typedef unsigned long long u64;
typedef unsigned short u16;
typedef u32 v4u __attribute__((ext_vector_type(4)));
typedef u32 v2u __attribute__((ext_vector_type(2)));
typedef float f4 __attribute__((ext_vector_type(4)));
typedef float f2 __attribute__((ext_vector_type(2)));
typedef _Float16 h2 __attribute__((ext_vector_type(2)));
typedef _Float16 h8 __attribute__((ext_vector_type(8)));
typedef __bf16 b2 __attribute__((ext_vector_type(2)));
typedef __bf16 b8 __attribute__((ext_vector_type(8)));
typedef short s4 __attribute__((ext_vector_type(4)));
#define LAS __attribute__((address_space(3)))
#define DEV __device__ __forceinline__

namespace {

constexpr int L = 2048, D = 640, NS = 192, F = 1536;
constexpr int NT = 768;
constexpr u32 SENT = 0xFFFFFFFFu;
constexpr u32 SPIN_CAP = 1u << 16;
#define DEP_L 4
#define DEP_M 4
#define DEP_H 4
#define P3_DELAY 0
#define P3_BACK 0
#define G3_DELAY 8
#define G3_BACK 0
#define ACT_DELAY 8
#define ACT_BACK 0
#define OUT_AUX 0
#define FFN_IDLE 4
#define HX_AUX 16
#define HROW_DELAY 0
#define CODE_KB 32
#define E1_DELAY 12

constexpr int WS_EPOCH = 0;
constexpr int WS_G1 = 4096;
constexpr int WS_G3 = WS_G1 + 8192;
constexpr int WS_IDX = WS_G3 + 16384;
constexpr int WS_ACT = WS_IDX + 2048;
constexpr int WS_UX = 65536;
constexpr int HXB = 216 * 64;
constexpr int WS_HX = WS_UX + L * D * 2;
constexpr int WS_HROW = WS_HX + 256 * HXB;
constexpr int WS_END = WS_HROW + L * D * 2;
constexpr int WS_DBG = 16 << 20;

constexpr int TCH = 800;
constexpr int TROW = 3 * TCH;
constexpr int LD_TBL = 0;
constexpr int KD_LEN = 672;
constexpr int LD_KD = 136800;
constexpr int LD_UT = LD_KD + 3 * KD_LEN * 2;
constexpr int LD_MISC = 153600;
constexpr int KCS = 4416;
constexpr int LD_KC = 0;
constexpr int ULEN = 2320;
constexpr int PT_G = 320;
constexpr int PT = 4 * PT_G;
constexpr int PT_CH = 19 * PT + 48;
constexpr int LD_PART = 0;
constexpr int LD_UU = 99328;
constexpr int LD_XR = 114688;
constexpr int LD_HT = 0;
constexpr int HT_ROW = 648 * 2;
constexpr int M_CLR = LD_MISC;
constexpr int M_CCNT = LD_MISC + 4;
constexpr int M_P1 = LD_MISC + 8;
constexpr int M_P3 = LD_MISC + 12;
constexpr int M_G3 = LD_MISC + 256;
constexpr int M_SCALE = LD_MISC + 16;
constexpr int M_WMAX = LD_MISC + 64;
constexpr int M_SEL = LD_MISC + 128;
constexpr int M_STAMP = LD_MISC + 512;
constexpr int Z0 = 2064;

constexpr int LF_W = 0;
constexpr int LF_CAND = 16384;
constexpr int LF_Z = 32768;
constexpr int LF_H = LF_Z + 8 * HT_ROW;
constexpr int LF_RED = 57344;
constexpr int LF_ACT = 73728;
constexpr int ACT_ROW = 3088;

DEV float bf_lo(u32 v) { return __uint_as_float(v << 16); }
DEV float bf_hi(u32 v) { return __uint_as_float(v & 0xffff0000u); }
DEV float bf2f(u16 v) { return __uint_as_float((u32)v << 16); }
DEV u32 pk_bf(float a, float b) {
    f2 v = {a, b};
    return __builtin_bit_cast(u32, __builtin_convertvector(v, b2));
}
DEV h2 as_h2(u32 v) { return __builtin_bit_cast(h2, v); }
DEV float rbf(float a) { return bf_lo(pk_bf(a, a)); }
DEV u32 pk_h(float a, float b) {
    f2 v = {a, b};
    return __builtin_bit_cast(u32, __builtin_convertvector(v, h2));
}
template <int CTRL> DEV float dppf(float v) {
    return __int_as_float(__builtin_amdgcn_update_dpp(0, __float_as_int(v), CTRL, 0xf, 0xf, true));
}
template <int CTRL> DEV u32 dppu(u32 v) { return (u32)__builtin_amdgcn_update_dpp(0, (int)v, CTRL, 0xf, 0xf, true); }
DEV float wave_sum(float v) {
    v += dppf<0xB1>(v);
    v += dppf<0x4E>(v);
    v += dppf<0x141>(v);
    v += dppf<0x140>(v);
    auto r = __builtin_amdgcn_permlane16_swap(__float_as_uint(v), __float_as_uint(v), false, false);
    v = __uint_as_float(r[0]) + __uint_as_float(r[1]);
    auto q = __builtin_amdgcn_permlane32_swap(__float_as_uint(v), __float_as_uint(v), false, false);
    return __uint_as_float(q[0]) + __uint_as_float(q[1]);
}
DEV u32 umax(u32 a, u32 b) { return a > b ? a : b; }
DEV u32 wave_max(u32 v) {
    v = umax(v, dppu<0xB1>(v));
    v = umax(v, dppu<0x4E>(v));
    v = umax(v, dppu<0x141>(v));
    v = umax(v, dppu<0x140>(v));
    auto r = __builtin_amdgcn_permlane16_swap(v, v, false, false);
    v = umax(r[0], r[1]);
    auto q = __builtin_amdgcn_permlane32_swap(v, v, false, false);
    return umax(q[0], q[1]);
}

typedef f2 cf;
DEV cf cmul(cf a, cf b) { return __builtin_elementwise_fma(f2{a.y, a.y}, f2{-b.y, b.x}, f2{a.x, a.x} * b); }
DEV cf csq(cf a) { return cmul(a, a); }
DEV cf cexp_pos(float zr, float zi, float pos) {
    const float mag = __builtin_amdgcn_exp2f(zr * pos * 1.4426950408889634f);
    const float rev = __builtin_amdgcn_fractf(zi * pos * 0.15915494309189535f);
    return f2{mag * __builtin_amdgcn_cosf(rev), mag * __builtin_amdgcn_sinf(rev)};
}
DEV u32 c2h(cf a) { return pk_h(a.x, a.y); }
DEV u32 hcmul(u32 a_, u32 b_) {
    const h2 a = __builtin_bit_cast(h2, a_), b = __builtin_bit_cast(h2, b_);
    const h2 t = h2{a.x, a.x} * b;
    const h2 r = __builtin_elementwise_fma(h2{a.y, a.y}, h2{-b.y, b.x}, t);
    return __builtin_bit_cast(u32, r);
}

DEV void wave_sum2(float& a, float& b) {
    a += dppf<0xB1>(a);  b += dppf<0xB1>(b);
    a += dppf<0x4E>(a);  b += dppf<0x4E>(b);
    a += dppf<0x141>(a); b += dppf<0x141>(b);
    a += dppf<0x140>(a); b += dppf<0x140>(b);
    auto r = __builtin_amdgcn_permlane16_swap(__float_as_uint(a), __float_as_uint(a), false, false);
    auto t = __builtin_amdgcn_permlane16_swap(__float_as_uint(b), __float_as_uint(b), false, false);
    a = __uint_as_float(r[0]) + __uint_as_float(r[1]);
    b = __uint_as_float(t[0]) + __uint_as_float(t[1]);
    auto q = __builtin_amdgcn_permlane32_swap(__float_as_uint(a), __float_as_uint(a), false, false);
    auto u = __builtin_amdgcn_permlane32_swap(__float_as_uint(b), __float_as_uint(b), false, false);
    a = __uint_as_float(q[0]) + __uint_as_float(q[1]);
    b = __uint_as_float(u[0]) + __uint_as_float(u[1]);
}
DEV void ln_stats(const u32 (&v)[5], float& m, float& s, float& rs) {
    const float c = bf_lo(__builtin_amdgcn_readfirstlane(v[0]));
    float s1 = 0.f, s2 = 0.f;
#pragma unroll
    for (int i = 0; i < 5; ++i) {
        const float a = bf_lo(v[i]) - c, b = bf_hi(v[i]) - c;
        s1 += a + b;
        s2 = __builtin_fmaf(a, a, __builtin_fmaf(b, b, s2));
    }
    wave_sum2(s1, s2);
    const float dm = s1 * (1.0f / D);
    const float var = __builtin_fmaxf(__builtin_fmaf(-dm, dm, s2 * (1.0f / D)), 0.f);
    m = rbf(c + dm);
    const float v2 = rbf(rbf(var) + 1e-5f);
    const float q0 = __builtin_amdgcn_sqrtf(v2);
    const float q1 = __builtin_fmaf(__builtin_fmaf(-q0, q0, v2), 0.5f * __builtin_amdgcn_rcpf(q0), q0);
    s = rbf(q1);
    rs = __builtin_amdgcn_rcpf(s);
}
DEV u32 ln_apply2(float x0, float x1, float m, float s, float rs) {
    const u32 a = pk_bf(x0 - m, x1 - m);
    const float a0 = bf_lo(a), a1 = bf_hi(a);
    const float q0 = a0 * rs, q1 = a1 * rs;
    return pk_bf(__builtin_fmaf(__builtin_fmaf(-q0, s, a0), rs, q0), __builtin_fmaf(__builtin_fmaf(-q1, s, a1), rs, q1));
}

template <int OFF>
DEV void wr8(u32 m0v, u32 a, u32 b, u32 c, u32 d, u32 e, u32 f, u32 g, u32 h) {
    asm volatile("s_mov_b32 m0, %8\n\ts_nop 0\n\t"
                 "ds_write_addtid_b32 %0 offset:%9\n\tds_write_addtid_b32 %1 offset:%10\n\t"
                 "ds_write_addtid_b32 %2 offset:%11\n\tds_write_addtid_b32 %3 offset:%12\n\t"
                 "ds_write_addtid_b32 %4 offset:%13\n\tds_write_addtid_b32 %5 offset:%14\n\t"
                 "ds_write_addtid_b32 %6 offset:%15\n\tds_write_addtid_b32 %7 offset:%16"
                 :: "v"(a), "v"(b), "v"(c), "v"(d), "v"(e), "v"(f), "v"(g), "v"(h), "s"(m0v), "i"(OFF), "i"(OFF + TROW),
                    "i"(OFF + 2 * TROW), "i"(OFF + 3 * TROW), "i"(OFF + 4 * TROW), "i"(OFF + 5 * TROW), "i"(OFF + 6 * TROW),
                    "i"(OFF + 7 * TROW) : "memory");
}
template <int OFF>
DEV void wr3(u32 m0v, u32 a, u32 b, u32 c) {
    asm volatile("s_mov_b32 m0, %3\n\ts_nop 0\n\t"
                 "ds_write_addtid_b32 %0 offset:%4\n\tds_write_addtid_b32 %1 offset:%5\n\tds_write_addtid_b32 %2 offset:%6"
                 :: "v"(a), "v"(b), "v"(c), "s"(m0v), "i"(OFF), "i"(OFF + TROW), "i"(OFF + 2 * TROW) : "memory");
}
DEV void lds_drain() { asm volatile("s_waitcnt lgkmcnt(0)" ::: "memory"); }
DEV v4u ld_sc1(__amdgpu_buffer_rsrc_t rs, int off) { return __builtin_amdgcn_raw_buffer_load_b128(rs, off, 0, 16); }
DEV void st_sc1(__amdgpu_buffer_rsrc_t rs, int off, v4u v) { __builtin_amdgcn_raw_buffer_store_b128(v, rs, off, 0, 16); }
DEV void drain() { asm volatile("s_waitcnt vmcnt(0)" ::: "memory"); }
DEV void vm_clean() { __builtin_amdgcn_s_waitcnt(0x0F70); }
DEV void cbar() { asm volatile("" ::: "memory"); }

constexpr u32 kidx(int lag) {
    if (lag < 0 || lag >= L) return 660u;
    const int sh = lag < 256 ? 0 : (lag < 512 ? 1 : (lag < 1024 ? 2 : 3));
    const int k = sh ? ((lag + (1 << (sh - 1)) - 1 + ((lag >> sh) & 1)) >> sh) : lag;
    return (u32)(128 * sh + k);
}
struct GatherTab { u32 v[66][8]; };
constexpr GatherTab make_gather_tab() {
    GatherTab t{};
    for (int i = 0; i < 66; ++i) {
        const int a = i < 65 ? 194 + i : 1;
        for (int k = 0; k < 6; ++k) t.v[i][k] = (kidx(Z0 - 8 * a - 2 * k) * 2) | (kidx(Z0 - 8 * a - 2 * k - 1) * 2 << 16);
    }
    return t;
}

template <int N0, int N1>
DEV void conv_load_frags(u32 kc, LAS const unsigned char* sm, int lane, h8 (&A)[29]) {
    const int q = lane & 15, g = lane >> 4;
    const int s0 = Z0 - q + 8 * g;
    const int sg = s0 & 3;
    u32 pl = kc + sg * KCS + 2 * (s0 - sg) - 64 * (N1 - 1);
    asm volatile("" : "+v"(pl));
#pragma unroll
    for (int n = N0; n < N1; ++n) {
        const v2u lo = *(LAS const v2u*)(sm + pl + 64 * (N1 - 1 - n));
        asm volatile("");
        const v2u hi = *(LAS const v2u*)(sm + pl + 64 * (N1 - 1 - n) + 8);
        asm volatile("");
        const v4u t = {lo.x, lo.y, hi.x, hi.y};
        A[n - N0] = __builtin_bit_cast(h8, t);
    }
}
template <int N0, int N1, int DEPTH>
DEV void conv_run(const h8 (&A)[29], LAS const unsigned char* ub_lo, f4 (&acc)[8]) {
    constexpr int NU_HI = N1 - 1 < 8 ? N1 - 1 : 8;
    constexpr int NU_LO = N0 - 56;
    constexpr int STEPS = NU_HI - NU_LO + 1;
    h8 B[DEPTH];
    constexpr int T0 = N0 <= 8 ? 0 : (N0 - 8 + 7) / 8;
#pragma unroll
    for (int T = T0; T < 8; ++T) asm volatile("" : "+v"(acc[T]));
    asm volatile("s_nop 4");
#pragma unroll
    for (int k = 0; k < DEPTH; ++k) B[k] = *(LAS const h8*)(ub_lo + 64 * (8 - (NU_HI - k)));
    __builtin_amdgcn_sched_barrier(0);
#pragma unroll
    for (int k = 0; k < STEPS; ++k) {
        const int nu = NU_HI - k;
#pragma unroll
        for (int T = 0; T < 8; ++T) {
            const int n = nu + 8 * T;
            if (n >= N0 && n < N1) asm volatile("v_mfma_f32_16x16x32_f16 %0, %1, %2, %0" : "+v"(acc[T]) : "v"(A[n - N0]), "v"(B[k % DEPTH]));
        }
        if (k + DEPTH < STEPS) B[k % DEPTH] = *(LAS const h8*)(ub_lo + 64 * (8 - (nu - DEPTH)));
        __builtin_amdgcn_sched_barrier(0);
    }
    asm volatile("s_nop 15\n\ts_nop 7");
#pragma unroll
    for (int T = T0; T < 8; ++T) asm volatile("" : "+v"(acc[T]));
}
DEV constexpr int pslot(int job, int T) { return job == 0 ? T : (job == 1 ? 7 + T : 11 + T); }

#define STAMP(i) do {} while (0)
#define PSTAMP(i) do {} while (0)
#define LSTAMP(i) STAMP(i)
#define STAMP_TID 0
}

__device__ const GatherTab gather_tab = make_gather_tab();

__global__ void __launch_bounds__(NT, 3)
s4d_moe_fused(const u16* __restrict__ x, const u16* __restrict__ pLogA, const u16* __restrict__ pAim,
              const u16* __restrict__ pCr, const u16* __restrict__ pCi, const u16* __restrict__ pLogDt,
              const u16* __restrict__ pDs, const u16* __restrict__ pWr, const u16* __restrict__ pWg,
              const u16* __restrict__ pWu, const u16* __restrict__ pWd, u16* __restrict__ out,
              unsigned char* __restrict__ ws) {
    __shared__ __attribute__((aligned(16))) unsigned char smem_[160 * 1024];
    LAS unsigned char* sm = (LAS unsigned char*)smem_;
    asm volatile("" :: "s"(x), "s"(pLogA), "s"(pAim), "s"(pCr), "s"(pCi), "s"(pLogDt), "s"(pDs), "s"(pWr), "s"(pWg), "s"(pWu), "s"(pWd),
                 "s"(out), "s"(ws));
    u64 pc0;
    asm volatile("s_getpc_b64 %0" : "=s"(pc0));
    const int tid = threadIdx.x, lane = tid & 63;
    const int w = __builtin_amdgcn_readfirstlane(tid >> 6);
    const int b = blockIdx.x, grp = b & 7, slot = b >> 3;
    const bool is_conv = slot < 27;
    const int d0 = 80 * grp + 3 * slot;
    const int nch = is_conv ? (slot == 26 ? 2 : 3) : 0;
    const int f = (slot - 27) * 8 + grp;
    const __amdgpu_buffer_rsrc_t rws = __builtin_amdgcn_make_buffer_rsrc((void*)ws, 0, WS_END, 0x00020000);

    STAMP(0);
    if (tid == 0) { *(LAS u32*)(sm + M_CLR) = 0; *(LAS u32*)(sm + M_CCNT) = 0; *(LAS u32*)(sm + M_P1) = 0; *(LAS u32*)(sm + M_P3) = 0; }

    u32 xrow[5], wr[5];
    if (w < 8) {
        const u32* xr = (const u32*)(x + (size_t)(8 * b + w) * D);
#pragma unroll
        for (int i = 0; i < 5; ++i) xrow[i] = xr[lane + 64 * i];
    }
    __syncthreads();
    u32 epoch = __builtin_amdgcn_raw_buffer_load_b32(rws, WS_EPOCH, 0, 0);
    auto get_tag = [&]() { u32 t = epoch; asm volatile("" : "+v"(t)); return t + 1u; };

    u32 tch[CODE_KB / 8 + 8];
#pragma unroll
    for (int i = 0; i < CODE_KB / 8 + 8; ++i) tch[i] = 0;
    auto touch_all = [&]() {
        typedef const __attribute__((address_space(1))) unsigned char* gptr;
        typedef const __attribute__((address_space(1))) u32* gptr32;
        const gptr pc = (gptr)(pc0 & ~127ull);
#pragma unroll
        for (int i = 0; i < CODE_KB / 8; ++i) tch[i] = *(gptr32)(pc + (i * 64 + lane) * 128);
#pragma unroll
        for (int i = 0; i < 3; ++i) tch[CODE_KB / 8 + i] = __builtin_amdgcn_raw_buffer_load_b32(rws, (i * 64 + lane) * 65536, 0, 0);
        tch[CODE_KB / 8 + 3] = *(const u32*)((const unsigned char*)out + min(lane * 65536, L * D * 2 - 4));
        tch[CODE_KB / 8 + 4] = *(const u32*)((const unsigned char*)pWg + min(lane * 32768, D * F * 2 - 4));
        tch[CODE_KB / 8 + 5] = *(const u32*)((const unsigned char*)pWu + min(lane * 32768, D * F * 2 - 4));
        tch[CODE_KB / 8 + 6] = *(const u32*)((const unsigned char*)pWd + min(lane * 32768, D * F * 2 - 4));
        tch[CODE_KB / 8 + 7] = *(const u32*)((const unsigned char*)pWr + (lane & 7) * 128);
    };
    auto touch_done = [&]() {
#pragma unroll
        for (int i = 0; i < CODE_KB / 8 + 8; ++i) asm volatile("" :: "v"(tch[i]));
    };

    auto p1_rows = [&]() {
        float m, s, rs;
        __builtin_amdgcn_s_setprio(3);
        ln_stats(xrow, m, s, rs);
#pragma unroll
        for (int i = 0; i < 5; ++i) {
            const u32 ub = ln_apply2(bf_lo(xrow[i]), bf_hi(xrow[i]), m, s, rs);
            *(LAS u32*)(sm + LD_UT + w * HT_ROW + (lane + 64 * i) * 4) = pk_h(bf_lo(ub), bf_hi(ub));
        }
        lds_drain();
        if (lane == 0) __hip_atomic_fetch_add((LAS u32*)(sm + M_P1), 1u, __ATOMIC_RELAXED, __HIP_MEMORY_SCOPE_WORKGROUP);
        __builtin_amdgcn_s_setprio(0);
        STAMP(1);
    };
    auto p1_store = [&]() {
        {
            {
                u32 sp = 0;
                while (__hip_atomic_load((LAS u32*)(sm + M_P1), __ATOMIC_RELAXED, __HIP_MEMORY_SCOPE_WORKGROUP) < 8u && ++sp < (1u << 22))
                    __builtin_amdgcn_s_sleep(1);
                cbar();
                PSTAMP(16);
            }
            auto tr = [&](int grp64) {
                const int cp = 64 * grp64 + (lane >> 1) + 32 * (lane & 1);
                u32 v[8];
#pragma unroll
                for (int k = 0; k < 8; ++k) v[k] = *(LAS const u32*)(sm + LD_UT + k * HT_ROW + cp * 4);
                const bool odd = lane & 1;
                u32 keep[4], recv[4];
#pragma unroll
                for (int k = 0; k < 4; ++k) {
                    const u32 lo = __builtin_amdgcn_perm(v[2 * k + 1], v[2 * k], 0x05040100u), hi = __builtin_amdgcn_perm(v[2 * k + 1], v[2 * k], 0x07060302u);
                    keep[k] = odd ? hi : lo;
                    recv[k] = dppu<0xB1>(odd ? lo : hi);
                }
                const v4u a = {odd ? recv[0] : keep[0], odd ? recv[1] : keep[1], odd ? recv[2] : keep[2], odd ? recv[3] : keep[3]};
                const v4u c = {odd ? keep[0] : recv[0], odd ? keep[1] : recv[1], odd ? keep[2] : recv[2], odd ? keep[3] : recv[3]};
                st_sc1(rws, WS_UX + b * (D * 16) + (128 * grp64 + lane) * 16, a);
                st_sc1(rws, WS_UX + b * (D * 16) + (128 * grp64 + 64 + lane) * 16, c);
            };
            tr(w - 9);
            if (w < 11) tr(w - 6);
            PSTAMP(18);
        }
    };
    auto p1_flag = [&]() {
        {
            drain();
            PSTAMP(19);
            u32 old = 0;
            if (lane == 0) old = __hip_atomic_fetch_add((LAS u32*)(sm + M_CLR), 1u, __ATOMIC_RELAXED, __HIP_MEMORY_SCOPE_WORKGROUP);
            old = __builtin_amdgcn_readfirstlane(old);
            if (old == 2u && lane < 8) __builtin_amdgcn_raw_buffer_store_b32(get_tag(), rws, WS_G1 + lane * 1024 + b * 4, 0, 16);
        }
    };
    auto clear_slots = [&]() {
        const v4u sv = {SENT, SENT, SENT, SENT};
        const int t = tid - 576;
#pragma unroll
        for (int i = 0; i < 4; ++i) st_sc1(rws, WS_HX + b * HXB + (t + 192 * i) * 16, sv);
        if (t < 96) st_sc1(rws, WS_HX + b * HXB + (768 + t) * 16, sv);
        if (w == 10) {
            if (lane >= 32 && lane < 36) st_sc1(rws, WS_G3 + b * 64 + (lane - 32) * 16, sv);
            if (!is_conv && f < 8 && lane >= 36 && lane < 38) st_sc1(rws, WS_IDX + f * 256 + (lane - 36) * 16, sv);
        }
        if (w == 11 && !is_conv && f < 24) st_sc1(rws, WS_ACT + (lane >> 3) * (F * 2) + f * 128 + (lane & 7) * 16, sv);
    };
    auto clear_rows = [&]() {
        const v4u sv = {SENT, SENT, SENT, SENT};
        const int t = tid - 576;
#pragma unroll
        for (int i = 0; i < 3; ++i) st_sc1(rws, WS_HROW + b * (D * 16) + (t + 192 * i) * 16, sv);
        if (w == 9) st_sc1(rws, WS_HROW + b * (D * 16) + (576 + lane) * 16, sv);
        drain();
    };
    if (w >= 9) clear_slots();

    v4u wf[20];
    if (is_conv) {
        u16 pla[3] = {0, 0, 0}, pai[3] = {0, 0, 0}, pcr[3] = {0, 0, 0}, pci[3] = {0, 0, 0};
        const int ad = min(d0 + (w < 9 ? w / 3 : w - 9), D - 1);
        const u16 pdt = pLogDt[ad];
        if (w < 9) {
            const int n = (w % 3) * 64 + lane;
            pla[0] = pLogA[ad * NS + n];
            pai[0] = pAim[ad * NS + n];
        } else {
#pragma unroll
            for (int i = 0; i < 3; ++i) {
                pla[i] = pLogA[ad * NS + i * 64 + lane];
                pai[i] = pAim[ad * NS + i * 64 + lane];
                pcr[i] = pCr[ad * NS + i * 64 + lane];
                pci[i] = pCi[ad * NS + i * 64 + lane];
            }
        }
        const int eo = tid / 3, ec = tid - 3 * eo;
        const u16 pdsk = pDs[min(d0 + min(tid & 3, 2), D - 1)];
        u32 gi[6], gi2[6];
        {
            const v4u* gt = (const v4u*)gather_tab.v[lane];
            const v4u g0 = gt[0], g1 = gt[1];
            gi[0] = g0.x; gi[1] = g0.y; gi[2] = g0.z; gi[3] = g0.w; gi[4] = g1.x; gi[5] = g1.y;
            const v4u* gu = (const v4u*)gather_tab.v[64 + (lane & 1)];
            const v4u g2 = gu[0], g3 = gu[1];
            gi2[0] = g2.x; gi2[1] = g2.y; gi2[2] = g2.z; gi2[3] = g2.w; gi2[4] = g3.x; gi2[5] = g3.y;
        }
        if (w == 8) touch_all();
        if (w < 8) p1_rows();
        if (w < 9) {
            const int c = w / 3;
            const u32 mb = LD_TBL + c * TCH + (w % 3) * 256;
            const float dt = rbf(__expf(bf2f(pdt)));
            const float zr = -rbf(__expf(bf2f(pla[0]))) * dt;
            const float zi = -bf2f(pai[0]) * dt;
            const cf B1 = cexp_pos(zr, zi, 1.f), H1 = cexp_pos(zr, zi, 128.f);
            const cf B2 = csq(B1), B3 = cmul(B2, B1), B4 = csq(B2), B5 = cmul(B4, B1), B6 = csq(B3), B7 = cmul(B4, B3);
            u32 hb[8];
            hb[0] = 0x00003C00u;
            hb[1] = c2h(B1); hb[2] = c2h(B2); hb[3] = c2h(B3); hb[4] = c2h(B4); hb[5] = c2h(B5); hb[6] = c2h(B6); hb[7] = c2h(B7);
            wr8<16 * TROW>(mb, hb[0], hb[1], hb[2], hb[3], hb[4], hb[5], hb[6], hb[7]);
            const cf H2 = csq(H1), H3 = cmul(H2, H1), H4 = csq(H2), H5 = cmul(H4, H1), H6 = csq(H3), H7 = cmul(H4, H3), H8 = csq(H4);
            const u32 h1 = c2h(H1), h2_ = c2h(H2), h3 = c2h(H3), h4 = c2h(H4), h5 = c2h(H5), h6 = c2h(H6), h7 = c2h(H7);
            wr3<24 * TROW>(mb, h1, hcmul(h1, hb[1]), hcmul(h1, hb[2]));
            const u32 mh = mb + 26 * TROW;
            wr8<1 * TROW>(mh, hcmul(h1, hb[3]), hcmul(h1, hb[4]), hcmul(h1, hb[5]), hcmul(h1, hb[6]), hcmul(h1, hb[7]),
                          h2_, hcmul(h2_, hb[2]), hcmul(h2_, hb[4]));
            wr8<9 * TROW>(mh, hcmul(h2_, hb[6]), h3, hcmul(h3, hb[2]), hcmul(h3, hb[4]), hcmul(h3, hb[6]),
                          h4, hcmul(h4, hb[4]), h5);
            wr8<17 * TROW>(mh, hcmul(h5, hb[4]), h6, hcmul(h6, hb[4]), h7, hcmul(h7, hb[4]), c2h(H8), c2h(cmul(H8, H1)),
                           c2h(csq(H5)));
            wr3<25 * TROW>(mh, c2h(cmul(H8, H3)), c2h(csq(H6)), c2h(cmul(H8, H5)));
            LAS u32* T = (LAS u32*)(sm + mb + lane * 4);
            T[54 * (TROW / 4)] = c2h(csq(H7));
            T[55 * (TROW / 4)] = c2h(cmul(H8, H7));
            T[56 * (TROW / 4)] = c2h(csq(H8));
            lds_drain();
        } else {
            __builtin_amdgcn_s_setprio(3);
            p1_store();
            p1_flag();
            __builtin_amdgcn_sched_barrier(0);
            const int c = w - 9;
            const float dt = rbf(__expf(bf2f(pdt)));
            float sabs = 0.f;
#pragma unroll
            for (int i = 0; i < 3; ++i) sabs += __builtin_fabsf(bf2f(pcr[i]) * dt) + __builtin_fabsf(bf2f(pci[i]) * dt);
            int e = 14 - __builtin_amdgcn_frexp_expf(wave_sum(sabs));
            e = e > 100 ? 100 : (e < -100 ? -100 : e);
            if (lane == 0) *(LAS int*)(sm + M_SCALE + 4 * c) = e;
            const float sc = __builtin_ldexpf(dt, e + 1);
            cf be[3], w8[3];
#pragma unroll
            for (int i = 0; i < 3; ++i) {
                w8[i] = cexp_pos(-rbf(__expf(bf2f(pla[i]))) * dt, bf2f(pai[i]) * dt, 8.f);
                be[i] = f2{bf2f(pcr[i]) * sc, bf2f(pci[i]) * sc};
            }
#pragma unroll
            for (int h = 0; h < 2; ++h) {
                u32 v[3][8];
#pragma unroll
                for (int j = 0; j < 8; ++j)
#pragma unroll
                    for (int i = 0; i < 3; ++i) {
                        v[i][j] = c2h(be[i]);
                        if (h == 0 || j < 7) be[i] = cmul(be[i], w8[i]);
                    }
#pragma unroll
                for (int i = 0; i < 3; ++i) {
                    const u32 mb = LD_TBL + c * TCH + i * 256;
                    if (h == 0) wr8<0>(mb, v[i][0], v[i][1], v[i][2], v[i][3], v[i][4], v[i][5], v[i][6], v[i][7]);
                    else wr8<8 * TROW>(mb, v[i][0], v[i][1], v[i][2], v[i][3], v[i][4], v[i][5], v[i][6], v[i][7]);
                }
            }
            if (lane < 16) *(LAS u16*)(sm + LD_KD + c * (KD_LEN * 2) + (656 + lane) * 2) = 0;
            lds_drain();
            __builtin_amdgcn_s_setprio(0);
        }
        touch_done();
        STAMP(2);
        __syncthreads();
        STAMP(3);
        if (w == 11) {
            const u32 tag = get_tag();
            __builtin_amdgcn_s_sleep(E1_DELAY);
            u32 sp = 0;
            while (true) {
                cbar();
                const v4u fl = ld_sc1(rws, WS_G1 + (b & 7) * 1024 + lane * 16);
                if (__all((fl.x == tag) & (fl.y == tag) & (fl.z == tag) & (fl.w == tag)) || ++sp > SPIN_CAP) break;
                __builtin_amdgcn_s_sleep(2);
            }
        }
        if (w < 9) {
            const int c = w / 3, tile = w % 3;
            const int j = lane & 15, g = lane >> 4;
            int row = 16 + 16 * tile + j;
            if (row > 56) row = 56;
            u32 pa = LD_TBL + c * TCH + row * TROW + g * 16, pb = LD_TBL + c * TCH + j * TROW + g * 16;
            asm volatile("" : "+v"(pa), "+v"(pb));
            h8 fa[12], fb[12];
#pragma unroll
            for (int t = 0; t < 12; ++t) {
                fa[t] = *(LAS const h8*)(sm + pa + 64 * t);
                fb[t] = *(LAS const h8*)(sm + pb + 64 * t);
            }
            __builtin_amdgcn_sched_barrier(0);
            f4 acc0 = {0.f, 0.f, 0.f, 0.f}, acc1 = {0.f, 0.f, 0.f, 0.f};
#pragma unroll
            for (int t = 0; t < 12; t += 2) {
                acc0 = __builtin_amdgcn_mfma_f32_16x16x32_f16(fa[t], fb[t], acc0, 0, 0, 0);
                acc1 = __builtin_amdgcn_mfma_f32_16x16x32_f16(fa[t + 1], fb[t + 1], acc1, 0, 0, 0);
            }
            const f4 acc = acc0 + acc1;
            const u32 p01 = pk_h(acc[0], acc[1]), p23 = pk_h(acc[2], acc[3]);
            LAS unsigned char* kd = sm + LD_KD + c * (KD_LEN * 2);
            if (tile == 0) {
                *(LAS v2u*)(kd + 2 * (128 * (g >> 1) + 4 * (g & 1) + 8 * j)) = v2u{p01, p23};
            } else if (tile == 1) {
                if (g < 2) {
                    *(LAS v2u*)(kd + 2 * (256 + 64 * g + 4 * j)) = v2u{p01, p23};
                } else {
                    *(LAS u32*)(kd + 2 * (384 + 64 * (g - 2) + 2 * j)) = p01;
                    *(LAS u32*)(kd + 2 * (384 + 64 * (g - 2) + 32 + 2 * j)) = p23;
                }
            } else {
                const int r0 = 4 * g;
                if (r0 <= 8) *(LAS u16*)(kd + 2 * (512 + 16 * r0 + j)) = (u16)p01;
                if (r0 + 3 <= 8) {
                    *(LAS u16*)(kd + 2 * (512 + 16 * (r0 + 1) + j)) = (u16)(p01 >> 16);
                    *(LAS u16*)(kd + 2 * (512 + 16 * (r0 + 2) + j)) = (u16)p23;
                    *(LAS u16*)(kd + 2 * (512 + 16 * (r0 + 3) + j)) = (u16)(p23 >> 16);
                }
            }
        }
        __syncthreads();
        cbar();
        const v4u uv = ld_sc1(rws, WS_UX + eo * (D * 16) + min(d0 + ec, D - 1) * 16);
        STAMP(4);
        {
            auto gather = [&](int c, const u32 (&ix)[6], u32 (&P)[6]) {
                LAS const unsigned char* kd = sm + LD_KD + c * (KD_LEN * 2);
#pragma unroll
                for (int k = 0; k < 6; ++k)
                    P[k] = (u32) * (LAS const u16*)(kd + (ix[k] & 0xffffu)) | ((u32) * (LAS const u16*)(kd + (ix[k] >> 16)) << 16);
            };
            auto put = [&](int c, int a, const u32 (&P)[6]) {
                LAS unsigned char* dst = sm + LD_KC + c * (4 * KCS) + a * 16;
                *(LAS v4u*)(dst) = v4u{P[0], P[1], P[2], P[3]};
                *(LAS v4u*)(dst + KCS) = v4u{__builtin_amdgcn_alignbit(P[1], P[0], 16), __builtin_amdgcn_alignbit(P[2], P[1], 16),
                                             __builtin_amdgcn_alignbit(P[3], P[2], 16), __builtin_amdgcn_alignbit(P[4], P[3], 16)};
                *(LAS v4u*)(dst + 2 * KCS) = v4u{P[1], P[2], P[3], P[4]};
                *(LAS v4u*)(dst + 3 * KCS) = v4u{__builtin_amdgcn_alignbit(P[2], P[1], 16), __builtin_amdgcn_alignbit(P[3], P[2], 16),
                                                 __builtin_amdgcn_alignbit(P[4], P[3], 16), __builtin_amdgcn_alignbit(P[5], P[4], 16)};
            };
            if (w < 8) {
#pragma unroll
                for (int i = 0; i < 5; ++i) *(LAS u32*)(sm + LD_XR + w * 1280 + (lane + 64 * i) * 4) = xrow[i];
            }
            const int cc = tid >> 8, ca = 2 + (tid & 255), kind = w & 3;
            LAS const unsigned char* kd = sm + LD_KD + cc * (KD_LEN * 2);
            u32 P[6], Q[6];
            if (kind < 2) {
                const int c0 = 642 - ca;
                const u32 v0 = *(LAS const u16*)(kd + 2 * c0), v1 = *(LAS const u16*)(kd + 2 * c0 - 2);
                const u32 w00 = v0 | (v0 << 16), w11 = v1 | (v1 << 16);
                P[0] = ca == 2 ? (v0 << 16) : w00;
                P[1] = w00;
                P[2] = (ca & 1) ? w11 : (v0 | (v1 << 16));
                P[3] = w11; P[4] = w11; P[5] = w11;
            } else if (kind == 2) {
                const int c0 = 772 - 2 * ca;
                const u32 hi = *(LAS const u32*)(kd + 2 * c0), lo = *(LAS const u32*)(kd + 2 * c0 - 4);
                P[0] = __builtin_amdgcn_perm(hi, hi, 0x01000100u);
                P[1] = __builtin_amdgcn_perm(hi, lo, 0x03020504u);
                P[2] = __builtin_amdgcn_perm(lo, lo, 0x03020302u);
                P[3] = __builtin_amdgcn_perm(lo, lo, 0x01000100u);
                P[4] = P[3]; P[5] = P[3];
            } else {
                gather(cc, gi, P);
            }
            if (w == 11) gather((lane >> 1) % 3, gi2, Q);
            put(cc, ca, P);
            if (w == 11) {
                if (lane < 6) put(lane >> 1, (lane & 1) ? 1 : 258, Q);
            } else if (w == 10) {
                for (int i = lane; i < 72; i += 64) {
                    const int cs = i / 6, k = i % 6;
                    *(LAS v4u*)(sm + LD_KC + (cs >> 2) * (4 * KCS) + (cs & 3) * KCS + (k == 0 ? 0 : 258 + k) * 16) = v4u{0, 0, 0, 0};
                }
            } else if (w == 9) {
                for (int i = lane; i < 102; i += 64) {
                    const int c = i / 34, k = i % 34;
                    const int el = k < 32 ? 8 * k : 2304 + 8 * (k - 32);
                    *(LAS v4u*)(sm + LD_UU + c * (ULEN * 2) + el * 2) = v4u{0, 0, 0, 0};
                }
            }
        }
        __syncthreads();
        STAMP(5);
        int job = -1, jc = 0;
        if ((w & 3) == 0) { job = 2; jc = w >> 2; }
        else if (w < 8) { job = (w >> 2); jc = (w & 3) - 1; }
        h8 A[29];
        const u32 kc = LD_KC + jc * (4 * KCS);
        if (job == 0) conv_load_frags<0, 16>(kc, sm, lane, A);
        else if (job == 1) conv_load_frags<16, 36>(kc, sm, lane, A);
        else if (job == 2) conv_load_frags<36, 65>(kc, sm, lane, A);
        STAMP(6);
        *(LAS v4u*)(sm + LD_UU + ec * (ULEN * 2) + (256 + 8 * eo) * 2) = uv;
        STAMP(7);
        __syncthreads();
        STAMP(8);
        if (job >= 0) {
            f4 acc[8];
#pragma unroll
            for (int T = 0; T < 8; ++T) acc[T] = f4{0.f, 0.f, 0.f, 0.f};
            const int p = lane & 15, g = lane >> 4;
            u32 uba = LD_UU + jc * (ULEN * 2) + (16 * p + 8 * g) * 2;
            asm volatile("" : "+v"(uba));
            LAS const unsigned char* ub = sm + uba;
            LAS unsigned char* part = sm + LD_PART + jc * PT_CH + g * PT_G + p * 16;
            if (job == 0) {
                conv_run<0, 16, DEP_L>(A, ub, acc);
#pragma unroll
                for (int T = 0; T < 8; ++T) *(LAS f4*)(part + pslot(0, T) * PT) = acc[T];
            } else if (job == 1) {
                conv_run<16, 36, DEP_M>(A, ub, acc);
#pragma unroll
                for (int T = 1; T < 8; ++T) *(LAS f4*)(part + pslot(1, T) * PT) = acc[T];
            } else {
                conv_run<36, 65, DEP_H>(A, ub, acc);
#pragma unroll
                for (int T = 4; T < 8; ++T) *(LAS f4*)(part + pslot(2, T) * PT) = acc[T];
            }
        } else {
            clear_rows();
        }
        STAMP(9);
        __syncthreads();
        STAMP(10);
        {
            const float dsk = bf2f(pdsk);
            const int pc = tid & 3, cc = pc < 3 ? pc : 2;
            const int e = *(LAS const int*)(sm + M_SCALE + 4 * cc);
            const float isc = __builtin_ldexpf(1.f, -e);
            auto piece = [&](int eo_, float (&y)[8]) {
                const int T = eo_ >> 5, p = (eo_ >> 1) & 15, g0 = (eo_ & 1) * 2;
                LAS const unsigned char* part = sm + LD_PART + cc * PT_CH + g0 * PT_G + p * 16;
                const int sM = T >= 1 ? pslot(1, T) : 0, sH = T >= 4 ? pslot(2, T) : 0;
                const float kM = T >= 1 ? 1.f : 0.f, kH = T >= 4 ? 1.f : 0.f;
                const f4 l0 = *(LAS const f4*)(part + T * PT), l1 = *(LAS const f4*)(part + T * PT + PT_G);
                const f4 m0 = *(LAS const f4*)(part + sM * PT), m1 = *(LAS const f4*)(part + sM * PT + PT_G);
                const f4 h0 = *(LAS const f4*)(part + sH * PT), h1 = *(LAS const f4*)(part + sH * PT + PT_G);
                const v4u ur = *(LAS const v4u*)(sm + LD_UU + cc * (ULEN * 2) + (256 + 8 * eo_) * 2);
                const f4 y0 = (l0 + m0 * kM + h0 * kH) * isc, y1 = (l1 + m1 * kM + h1 * kH) * isc;
#pragma unroll
                for (int r = 0; r < 4; ++r) { y[r] = y0[r]; y[4 + r] = y1[r]; }
                const u32 uu[4] = {ur.x, ur.y, ur.z, ur.w};
                u32 o[4];
#pragma unroll
                for (int k = 0; k < 4; ++k) {
                    const h2 u2 = as_h2(uu[k]);
                    const u32 yb = pk_bf(y[2 * k], y[2 * k + 1]), ud = pk_bf((float)u2.x * dsk, (float)u2.y * dsk);
                    o[k] = pk_bf(bf_lo(yb) + bf_lo(ud), bf_hi(yb) + bf_hi(ud));
                }
                const bool real = pc < nch;
                return v4u{real ? o[0] : 0u, real ? o[1] : 0u, real ? o[2] : 0u, real ? o[3] : 0u};
            };
            const int pid = grp * 27 + slot;
            float ya[8], yb_[8];
            const int o1 = tid >> 2, o2 = 192 + ((tid - 512) >> 2);
            const v4u va = piece(o1, ya);
            v4u vb;
            if (w >= 8) vb = piece(o2, yb_);
            __builtin_amdgcn_raw_buffer_store_b128(va, rws, WS_HX + o1 * HXB + pid * 64 + pc * 16, 0, HX_AUX);
            if (w >= 8) __builtin_amdgcn_raw_buffer_store_b128(vb, rws, WS_HX + o2 * HXB + pid * 64 + pc * 16, 0, HX_AUX);
            STAMP(11);
            drain();
        }
    } else {
        v4u rw[16];
        auto load_weights = [&]() {
            if (f < 24) {
#pragma unroll
                for (int i = 0; i < 7; ++i) {
                    const int p = min(tid + NT * i, 5119);
                    rw[i] = *(const v4u*)(pWg + (size_t)(p >> 3) * F + 64 * f + 8 * (p & 7));
                    rw[8 + i] = *(const v4u*)(pWu + (size_t)(p >> 3) * F + 64 * f + 8 * (p & 7));
                }
            } else if (f < 34) {
#pragma unroll
                for (int i = 0; i < 16; ++i) {
                    const int p = tid + NT * i;
                    rw[i] = *(const v4u*)(pWd + (size_t)(p >> 3) * D + 64 * (f - 24) + 8 * (p & 7));
                }
            }
        };
        if (w == 8) touch_all();
        if (w < 8) p1_rows();
        if (w >= 9) { __builtin_amdgcn_s_setprio(3); p1_store(); p1_flag(); __builtin_amdgcn_s_setprio(0); }
        touch_done();
        load_weights();
        const int g = lane >> 4, q = (lane & 15) >> 2, pp = lane & 3;
        if (f < 24) {
#pragma unroll
            for (int rnd = 0; rnd < 2; ++rnd) {
#pragma unroll
                for (int i = 0; i < 7; ++i) {
                    const int p = tid + NT * i;
                    if (p < 5120) *(LAS v4u*)(sm + LF_W + p * 16) = rw[8 * rnd + i];
                }
                __syncthreads();
                if (w < 8 && (w >> 2) == rnd) {
                    LAS const unsigned char* base = sm + LF_W + (w & 3) * 32 + pp * 8;
#pragma unroll
                    for (int s = 0; s < 20; ++s) {
                        const int k0 = 32 * s + 8 * g + q;
                        const s4 a = __builtin_amdgcn_ds_read_tr16_b64_v4i16((LAS s4*)(base + k0 * 128));
                        const s4 c = __builtin_amdgcn_ds_read_tr16_b64_v4i16((LAS s4*)(base + (k0 + 4) * 128));
                        const v2u a2 = __builtin_bit_cast(v2u, a), c2 = __builtin_bit_cast(v2u, c);
                        wf[s] = v4u{a2.x, a2.y, c2.x, c2.y};
                    }
                }
                __syncthreads();
            }
        } else if (f < 34) {
#pragma unroll
            for (int rnd = 0; rnd < 2; ++rnd) {
#pragma unroll
                for (int i = 0; i < 8; ++i) *(LAS v4u*)(sm + LF_W + (tid + NT * i) * 16) = rw[8 * rnd + i];
                __syncthreads();
                LAS const unsigned char* base = sm + LF_W + (w & 3) * 32 + pp * 8;
#pragma unroll
                for (int s = 0; s < 16; ++s) {
                    const int ks = 16 * (w >> 2) + s;
                    if ((ks >= 24) == (rnd == 1)) {
                        const int k0 = 32 * (ks - 24 * rnd) + 8 * g + q;
                        const s4 a = __builtin_amdgcn_ds_read_tr16_b64_v4i16((LAS s4*)(base + k0 * 128));
                        const s4 c = __builtin_amdgcn_ds_read_tr16_b64_v4i16((LAS s4*)(base + (k0 + 4) * 128));
                        const v2u a2 = __builtin_bit_cast(v2u, a), c2 = __builtin_bit_cast(v2u, c);
                        wf[s] = v4u{a2.x, a2.y, c2.x, c2.y};
                    }
                }
                __syncthreads();
            }
        }
        if (w >= 9) clear_rows();
        if (w < 8) {
#pragma unroll
            for (int i = 0; i < 5; ++i) *(LAS u32*)(sm + LD_XR + w * 1280 + (lane + 64 * i) * 4) = xrow[i];
        }
        __syncthreads();
        if (w == 0) {
            u32 sp = 0;
            while (true) {
                cbar();
                const u32 v = __builtin_amdgcn_raw_buffer_load_b32(rws, WS_HX + b * HXB, 0, 16);
                if (v != SENT || ++sp > SPIN_CAP) break;
                __builtin_amdgcn_s_sleep(FFN_IDLE);
            }
        }
    }
    if (w < 8) {
#pragma unroll
        for (int i = 0; i < 5; ++i) wr[i] = ((const u32*)pWr)[lane + 64 * i];
    }
    vm_clean();
    __syncthreads();
    STAMP(12);

    if (tid < D) {
        v4u v;
        u32 sp = 0;
        const int cg = tid / 80, cr = tid - 80 * cg, cs = cr / 3;
        const int hxo = WS_HX + b * HXB + (cg * 27 + cs) * 64 + (cr - 3 * cs) * 16;
        if (P3_DELAY && is_conv) __builtin_amdgcn_s_sleep(P3_DELAY);
        while (true) {
            cbar();
            v = ld_sc1(rws, hxo);
            const bool ok = (v.x != SENT) & (v.y != SENT) & (v.z != SENT) & (v.w != SENT);
            if (__all(ok) || ++sp > SPIN_CAP) break;
            if (P3_BACK) __builtin_amdgcn_s_sleep(P3_BACK);
        }
        STAMP(13);
        const bool odd = tid & 1;
        const u32 s0 = odd ? v.x : v.z, s1 = odd ? v.y : v.w;
        const u32 r0 = dppu<0xB1>(s0), r1 = dppu<0xB1>(s1);
        const u32 k0 = odd ? v.z : v.x, k1 = odd ? v.w : v.y;
        const u32 lo0 = odd ? r0 : k0, hi0 = odd ? k0 : r0, lo1 = odd ? r1 : k1, hi1 = odd ? k1 : r1;
        LAS u32* dst = (LAS u32*)(sm + LD_HT + (odd ? 4 * HT_ROW : 0) + (tid >> 1) * 4);
        dst[0 * (HT_ROW / 4)] = __builtin_amdgcn_perm(hi0, lo0, 0x05040100u);
        dst[1 * (HT_ROW / 4)] = __builtin_amdgcn_perm(hi0, lo0, 0x07060302u);
        dst[2 * (HT_ROW / 4)] = __builtin_amdgcn_perm(hi1, lo1, 0x05040100u);
        dst[3 * (HT_ROW / 4)] = __builtin_amdgcn_perm(hi1, lo1, 0x07060302u);
    }
    __syncthreads();
    u32 orow[5];
    const int mytok = 8 * b + w;
    if (w < 8) {
        u32 hv[5];
#pragma unroll
        for (int i = 0; i < 5; ++i) {
            const u32 a = *(LAS const u32*)(sm + LD_HT + w * HT_ROW + (lane + 64 * i) * 4);
            const u32 xv = *(LAS const u32*)(sm + LD_XR + w * 1280 + (lane + 64 * i) * 4);
            hv[i] = pk_bf(bf_lo(xv) + bf_lo(a), bf_hi(xv) + bf_hi(a));
        }
        float m, s, rs;
        ln_stats(hv, m, s, rs);
        float racc = 0.f;
#pragma unroll
        for (int i = 0; i < 5; ++i) {
            const u32 z = ln_apply2(bf_lo(hv[i]), bf_hi(hv[i]), m, s, rs);
            racc = __builtin_fmaf(bf_lo(z), bf_lo(wr[i]), __builtin_fmaf(bf_hi(z), bf_hi(wr[i]), racc));
            orow[i] = pk_bf(bf_lo(hv[i]) + bf_lo(z), bf_hi(hv[i]) + bf_hi(z));
        }
        const float r = wave_sum(racc);
        u32 old = 0;
        if (lane == 0) {
            *(LAS v2u*)(sm + M_G3 + w * 8) = v2u{(__float_as_uint(m) >> 16) | (__float_as_uint(s) & 0xffff0000u), pk_bf(r, 0.f) & 0xffffu};
            lds_drain();
            old = __hip_atomic_fetch_add((LAS u32*)(sm + M_P3), 1u, __ATOMIC_RELAXED, __HIP_MEMORY_SCOPE_WORKGROUP);
        }
        old = __builtin_amdgcn_readfirstlane(old);
        if (old == 7u && lane < 4) {
            cbar();
            st_sc1(rws, WS_G3 + b * 64 + lane * 16, *(LAS const v4u*)(sm + M_G3 + lane * 16));
        }
        STAMP(14);
    } else {
        if (HROW_DELAY) __builtin_amdgcn_s_sleep(HROW_DELAY);
        const int t = tid - 512;
        v4u a[3], xv[3];
#pragma unroll
        for (int i = 0; i < 3; ++i) {
            const int p = min(t + 256 * i, 639), k = p / 80, ch = p - 80 * k;
            a[i] = *(LAS const v4u*)(sm + LD_HT + k * HT_ROW + ch * 16);
            xv[i] = *(LAS const v4u*)(sm + LD_XR + k * 1280 + ch * 16);
        }
#pragma unroll
        for (int i = 0; i < 3; ++i) {
            const v4u h = {pk_bf(bf_lo(xv[i].x) + bf_lo(a[i].x), bf_hi(xv[i].x) + bf_hi(a[i].x)),
                           pk_bf(bf_lo(xv[i].y) + bf_lo(a[i].y), bf_hi(xv[i].y) + bf_hi(a[i].y)),
                           pk_bf(bf_lo(xv[i].z) + bf_lo(a[i].z), bf_hi(xv[i].z) + bf_hi(a[i].z)),
                           pk_bf(bf_lo(xv[i].w) + bf_lo(a[i].w), bf_hi(xv[i].w) + bf_hi(a[i].w))};
            if (i < 2 || w < 10) st_sc1(rws, WS_HROW + b * (D * 16) + (t + 256 * i) * 16, h);
        }
    }
    STAMP(15);

    if (!is_conv && f < 34) {
        u32 key[4], msv[4];
        if (w < 8) {
            __builtin_amdgcn_s_sleep(G3_DELAY);
            v4u g0, g1;
            u32 sp = 0;
            while (true) {
                cbar();
                g0 = ld_sc1(rws, WS_G3 + tid * 32);
                g1 = ld_sc1(rws, WS_G3 + tid * 32 + 16);
                const bool ok = (g0.y != SENT) & (g0.w != SENT) & (g1.y != SENT) & (g1.w != SENT);
                if (__all(ok) || ++sp > SPIN_CAP) break;
                if (G3_BACK) __builtin_amdgcn_s_sleep(G3_BACK);
            }
            LSTAMP(16);
            const u32 rb[4] = {g0.y, g0.w, g1.y, g1.w};
            msv[0] = g0.x; msv[1] = g0.z; msv[2] = g1.x; msv[3] = g1.z;
#pragma unroll
            for (int j = 0; j < 4; ++j) {
                const u32 o = (rb[j] & 0x8000u) ? (~rb[j] & 0xffffu) : (rb[j] | 0x8000u);
                key[j] = (o << 11) | (u32)(2047 - (4 * tid + j));
            }
            const u32 mx = wave_max(umax(umax(key[0], key[1]), umax(key[2], key[3])));
            if (lane == 0) *(LAS u32*)(sm + M_WMAX + 4 * w) = mx;
        }
        __syncthreads();
        if (w < 8) {
            const v4u a = *(LAS const v4u*)(sm + M_WMAX), c = *(LAS const v4u*)(sm + M_WMAX + 16);
            const u32 thr = min(min(min(a.x, a.y), min(a.z, a.w)), min(min(c.x, c.y), min(c.z, c.w)));
            u64 bal[4];
            int tot = 0;
#pragma unroll
            for (int j = 0; j < 4; ++j) { bal[j] = __ballot(key[j] >= thr); tot += __builtin_popcountll(bal[j]); }
            u32 base = 0;
            if (lane == 0) base = __hip_atomic_fetch_add((LAS u32*)(sm + M_CCNT), (u32)tot, __ATOMIC_RELAXED, __HIP_MEMORY_SCOPE_WORKGROUP);
            base = __builtin_amdgcn_readfirstlane(base);
#pragma unroll
            for (int j = 0; j < 4; ++j) {
                const u32 pos = base + __builtin_amdgcn_mbcnt_hi((u32)(bal[j] >> 32), __builtin_amdgcn_mbcnt_lo((u32)bal[j], 0));
                if (key[j] >= thr) *(LAS v2u*)(sm + LF_CAND + pos * 8) = v2u{key[j], msv[j]};
                base += __builtin_popcountll(bal[j]);
            }
        }
        __syncthreads();
        {
            const u32 cnt = *(LAS const u32*)(sm + M_CCNT);
            for (u32 i0 = 64 * w; i0 < cnt; i0 += NT) {
                const u32 i = i0 + lane;
                const v2u me = *(LAS const v2u*)(sm + LF_CAND + min(i, cnt - 1) * 8);
                u32 rank = 0;
                for (u32 j0 = 0; j0 < cnt; j0 += 64) {
                    const u32 ot = (j0 + lane) < cnt ? *(LAS const u32*)(sm + LF_CAND + (j0 + lane) * 8) : 0u;
                    const u32 nj = min(64u, cnt - j0);
                    for (u32 j = 0; j < nj; j += 4) {
                        rank += (u32)__builtin_amdgcn_readlane(ot, j) > me.x;
                        rank += (u32)__builtin_amdgcn_readlane(ot, j + 1) > me.x;
                        rank += (u32)__builtin_amdgcn_readlane(ot, j + 2) > me.x;
                        rank += (u32)__builtin_amdgcn_readlane(ot, j + 3) > me.x;
                    }
                }
                if (i < cnt && rank < 8) {
                    const u32 rbits = me.x >> 11;
                    const u32 rb = (rbits & 0x8000u) ? (rbits & 0x7fffu) : (~rbits & 0xffffu);
                    *(LAS v4u*)(sm + M_SEL + rank * 16) = v4u{2047u - (me.x & 2047u), me.y, rb, 0u};
                }
            }
        }
        __syncthreads();
        LSTAMP(17);
        if (tid < D) {
            const int k = tid / 80, ch = tid % 80;
            const v4u sel = *(LAS const v4u*)(sm + M_SEL + k * 16);
            v4u hv;
            u32 sp = 0;
            while (true) {
                cbar();
                hv = ld_sc1(rws, WS_HROW + (int)(sel.x & (L - 1)) * (D * 2) + ch * 16);
                const bool ok = (hv.x != SENT) & (hv.y != SENT) & (hv.z != SENT) & (hv.w != SENT);
                if (__all(ok) || ++sp > SPIN_CAP) break;
            }
            const float m = bf_lo(sel.y), s = bf_hi(sel.y), rs = __builtin_amdgcn_rcpf(s);
            const u32 hh[4] = {hv.x, hv.y, hv.z, hv.w};
            u32 zz[4];
#pragma unroll
            for (int i = 0; i < 4; ++i) zz[i] = ln_apply2(bf_lo(hh[i]), bf_hi(hh[i]), m, s, rs);
            *(LAS v4u*)(sm + LF_Z + k * HT_ROW + ch * 16) = v4u{zz[0], zz[1], zz[2], zz[3]};
            *(LAS v4u*)(sm + LF_H + k * HT_ROW + ch * 16) = hv;
        } else if (w == 11 && f < 8 && lane < 2) {
            const v4u s0 = *(LAS const v4u*)(sm + M_SEL + lane * 64), s1 = *(LAS const v4u*)(sm + M_SEL + lane * 64 + 16),
                      s2 = *(LAS const v4u*)(sm + M_SEL + lane * 64 + 32), s3 = *(LAS const v4u*)(sm + M_SEL + lane * 64 + 48);
            st_sc1(rws, WS_IDX + f * 256 + lane * 16, v4u{s0.x, s1.x, s2.x, s3.x});
        }
        __syncthreads();
        LSTAMP(18);
        const int row = lane & 7, g = lane >> 4;
        if (f < 24) {
            f4 acc = {0.f, 0.f, 0.f, 0.f};
            if (w < 8) {
                f4 acc1 = {0.f, 0.f, 0.f, 0.f};
                LAS const unsigned char* zb = sm + LF_Z + row * HT_ROW + 16 * g;
                b8 za[10];
#pragma unroll
                for (int s = 0; s < 10; ++s) za[s] = *(LAS const b8*)(zb + 64 * s);
                __builtin_amdgcn_sched_barrier(0);
#pragma unroll
                for (int s = 0; s < 20; s += 2) {
                    acc = __builtin_amdgcn_mfma_f32_16x16x32_bf16(za[s % 10], __builtin_bit_cast(b8, wf[s]), acc, 0, 0, 0);
                    acc1 = __builtin_amdgcn_mfma_f32_16x16x32_bf16(za[(s + 1) % 10], __builtin_bit_cast(b8, wf[s + 1]), acc1, 0, 0, 0);
                    if (s < 10) {
                        za[s] = *(LAS const b8*)(zb + 64 * (s + 10));
                        za[s + 1] = *(LAS const b8*)(zb + 64 * (s + 11));
                    }
                    __builtin_amdgcn_sched_barrier(0);
                }
                acc += acc1;
                if (w >= 4) *(LAS f4*)(sm + LF_RED + (w - 4) * 1024 + lane * 16) = acc;
            }
            __syncthreads();
            if (w < 4) {
                const f4 up = *(LAS const f4*)(sm + LF_RED + w * 1024 + lane * 16);
                float a[4];
#pragma unroll
                for (int r = 0; r < 4; ++r) {
                    const float gb = rbf(acc[r]);
                    const float sg = rbf(gb * __builtin_amdgcn_rcpf(1.f + __expf(-gb)));
                    a[r] = sg * rbf(up[r]);
                }
                if (g < 2) {
#pragma unroll
                    for (int r = 0; r < 4; r += 2) {
                        const u32 pr = pk_bf(a[r], a[r + 1]);
                        *(LAS u16*)(sm + LF_ACT + (4 * g + r) * 128 + (16 * w + (lane & 15)) * 2) = (u16)pr;
                        *(LAS u16*)(sm + LF_ACT + (4 * g + r + 1) * 128 + (16 * w + (lane & 15)) * 2) = (u16)(pr >> 16);
                    }
                }
                lds_drain();
                u32 old = 0;
                if (lane == 0) old = __hip_atomic_fetch_add((LAS u32*)(sm + M_P1), 1u, __ATOMIC_RELAXED, __HIP_MEMORY_SCOPE_WORKGROUP);
                old = __builtin_amdgcn_readfirstlane(old);
                if (old == 8u + 3u) {
                    cbar();
                    st_sc1(rws, WS_ACT + (lane >> 3) * (F * 2) + f * 128 + (lane & 7) * 16, *(LAS const v4u*)(sm + LF_ACT + lane * 16));
                }
            }
            LSTAMP(19);
        } else {
            {
                v4u a0, a1;
                u32 sp = 0;
                __builtin_amdgcn_s_sleep(ACT_DELAY);
                while (true) {
                    cbar();
                    a0 = ld_sc1(rws, WS_ACT + tid * 16);
                    a1 = ld_sc1(rws, WS_ACT + (tid + NT) * 16);
                    const bool ok = (a0.x != SENT) & (a0.y != SENT) & (a0.z != SENT) & (a0.w != SENT) & (a1.x != SENT) &
                                    (a1.y != SENT) & (a1.z != SENT) & (a1.w != SENT);
                    if (__all(ok) || ++sp > SPIN_CAP) break;
                    if (ACT_BACK) __builtin_amdgcn_s_sleep(ACT_BACK);
                }
                *(LAS v4u*)(sm + LF_ACT + (tid / 192) * ACT_ROW + (tid % 192) * 16) = a0;
                *(LAS v4u*)(sm + LF_ACT + ((tid + NT) / 192) * ACT_ROW + ((tid + NT) % 192) * 16) = a1;
            }
            __syncthreads();
            LSTAMP(20);
            f4 acc = {0.f, 0.f, 0.f, 0.f}, acc1 = {0.f, 0.f, 0.f, 0.f};
            LAS const unsigned char* ab = sm + LF_ACT + row * ACT_ROW + 1024 * (w >> 2) + 16 * g;
            b8 aa[16];
#pragma unroll
            for (int s = 0; s < 16; ++s) aa[s] = *(LAS const b8*)(ab + 64 * s);
            __builtin_amdgcn_sched_barrier(0);
#pragma unroll
            for (int s = 0; s < 16; s += 2) {
                acc = __builtin_amdgcn_mfma_f32_16x16x32_bf16(aa[s], __builtin_bit_cast(b8, wf[s]), acc, 0, 0, 0);
                acc1 = __builtin_amdgcn_mfma_f32_16x16x32_bf16(aa[s + 1], __builtin_bit_cast(b8, wf[s + 1]), acc1, 0, 0, 0);
            }
            acc += acc1;
            *(LAS f4*)(sm + LF_RED + w * 1024 + lane * 16) = acc;
            __syncthreads();
            if (tid < 512) {
                const int tok = tid >> 6, col = tid & 63, tile = col >> 4;
                const int off = ((tok >> 2) * 16 + (col & 15)) * 16 + (tok & 3) * 4;
                const float y = *(LAS const float*)(sm + LF_RED + tile * 1024 + off) + *(LAS const float*)(sm + LF_RED + (4 + tile) * 1024 + off) +
                                *(LAS const float*)(sm + LF_RED + (8 + tile) * 1024 + off);
                const v4u sel = *(LAS const v4u*)(sm + M_SEL + tok * 16);
                const int d = 64 * (f - 24) + col;
                const float z = bf2f(*(LAS const u16*)(sm + LF_Z + tok * HT_ROW + d * 2));
                const float h = bf2f(*(LAS const u16*)(sm + LF_H + tok * HT_ROW + d * 2));
                const float r = bf2f((u16)sel.z);
                const float sg = rbf(__builtin_amdgcn_rcpf(1.f + __expf(-r)));
                const float upd = rbf(rbf(y) * sg);
                out[(size_t)(sel.x & (L - 1)) * D + d] = (u16)pk_bf(h + rbf(z + upd), 0.f);
            }
        }
    }

    STAMP(21);
    if (w < 8) {
        v4u i0, i1;
        u32 sp = 0;
        while (true) {
            cbar();
            i0 = ld_sc1(rws, WS_IDX + (b & 7) * 256);
            i1 = ld_sc1(rws, WS_IDX + (b & 7) * 256 + 16);
            const bool ok = (i0.x != SENT) & (i0.y != SENT) & (i0.z != SENT) & (i0.w != SENT) & (i1.x != SENT) & (i1.y != SENT) &
                            (i1.z != SENT) & (i1.w != SENT);
            if (__all(ok) || ++sp > SPIN_CAP) break;
            __builtin_amdgcn_s_sleep(16);
        }
        const u32 t = (u32)mytok;
        const bool routed = (i0.x == t) | (i0.y == t) | (i0.z == t) | (i0.w == t) | (i1.x == t) | (i1.y == t) | (i1.z == t) | (i1.w == t);
        if (!routed) {
            const __amdgpu_buffer_rsrc_t ro = __builtin_amdgcn_make_buffer_rsrc((void*)out, 0, L * D * 2, 0x00020000);
#pragma unroll
            for (int i = 0; i < 5; ++i) __builtin_amdgcn_raw_buffer_store_b32(orow[i], ro, mytok * (D * 2) + (lane + 64 * i) * 4, 0, OUT_AUX);
        }
        if (b == 0 && tid == 0) *(u32*)(ws + WS_EPOCH) = get_tag();
    }
}

extern "C" void kernel_launch(void* const* d_in, const int* in_sizes, int n_in, void* d_out, int out_size, void* d_ws,
                              size_t ws_size, hipStream_t stream) {
    (void)in_sizes; (void)n_in; (void)out_size; (void)ws_size;
    s4d_moe_fused<<<256, NT, 0, stream>>>((const u16*)d_in[0], (const u16*)d_in[1], (const u16*)d_in[2], (const u16*)d_in[3],
                                          (const u16*)d_in[4], (const u16*)d_in[5], (const u16*)d_in[6],
                                          (const u16*)d_in[8], (const u16*)d_in[9], (const u16*)d_in[10],
                                          (const u16*)d_in[11], (u16*)d_out, (unsigned char*)d_ws);
}
```
